```python
import torch

def _softmax(x, axis=-1):
    return torch.softmax(x, dim=axis)

def _gelu(x):
    return torch.nn.functional.gelu(x, approximate='tanh')

def _silu(x):
    return torch.nn.functional.silu(x)

def _sigmoid(x):
    return torch.sigmoid(x)

def _layernorm(x, axis=-1, eps=1e-05):
    m = x.mean(dim=axis, keepdim=True)
    v = x.var(dim=axis, keepdim=True, unbiased=False)
    return (x - m) / torch.sqrt(v + eps)

def _scatter_add(tgt, idx, src, axis=0):
    return tgt.clone().index_add_(axis, idx, src)

def _segment_sum(src, seg, n):
    out = torch.zeros((n,) + src.shape[1:], dtype=src.dtype)
    return out.index_add_(0, seg, src)

def _pad(x, pw):
    flat = tuple((v for pair in reversed(pw) for v in pair))
    return torch.nn.functional.pad(x, flat)
kernel_name = 'adaLN_cond__sliding_local_dilated__dense_activation_silu__prompt'

def setup_inputs():

    _w = lambda a: (lambda t: t.to(torch.bfloat16) if t.is_floating_point() else t)(torch.as_tensor(a))

    def _A_setup():
        B, S, D = (1, 2048, 448)
        return {'x': _w((lambda *s: torch.randn(*s, dtype=torch.bfloat16) * 0.05)(B, S, D)), 'wqkv': _w((lambda *s: torch.randn(*s, dtype=torch.bfloat16) * 0.05)(D, 3 * D)), 'wo': _w((lambda *s: torch.randn(*s, dtype=torch.bfloat16) * 0.05)(D, D))}
    _A_in = _A_setup()

    def _B_setup():
        B, S, D, F = (1, 2048, 448, 1792)
        return {'x': _w((lambda *s: torch.randn(*s, dtype=torch.bfloat16) * 0.05)(B, S, D)), 'w1': _w((lambda *s: torch.randn(*s, dtype=torch.bfloat16) * 3.2)(D, F) * D ** (-0.5)), 'w2': _w((lambda *s: torch.randn(*s, dtype=torch.bfloat16) * 0.4)(F, D) * F ** (-0.5))}
    _B_in = _B_setup()
    return {'A_x': _A_in['x'], 'A_wqkv': _A_in['wqkv'], 'A_wo': _A_in['wo'], 'B_x': _B_in['x'], 'B_w1': _B_in['w1'], 'B_w2': _B_in['w2'], 'c': _w((lambda *s: torch.randn(*s, dtype=torch.bfloat16) * 0.1)(1, 448) * 8.0), 'w_mod': _w((lambda *s: torch.randn(*s, dtype=torch.bfloat16) * 0.1)(448, 6 * 448) * 448 ** (-0.5) * 8.0)}

def reference(A_x, A_wqkv, A_wo, B_x, B_w1, B_w2, c, w_mod):

    def _A_ref(x, wqkv, wo):
        B, S, D = x.shape
        H = 16
        Dh = D // H
        W, dil = (8, 2)
        qkv = (lambda x, *d: x.permute(*d))((x @ wqkv).reshape(B, S, 3, H, Dh), 2, 0, 3, 1, 4)
        q, k, v = (qkv[0], qkv[1], qkv[2])
        scale = Dh ** (-0.5)
        pos = torch.arange(S)
        off = torch.arange(W) * dil
        idx = pos[:, None] - off[None, :]
        mask = idx >= 0
        idx_c = torch.where(mask, idx, 0).reshape(-1)
        kg = k[:, :, idx_c, :].reshape(B, H, S, W, Dh)
        vg = v[:, :, idx_c, :].reshape(B, H, S, W, Dh)
        logits = torch.einsum('bhsd,bhswd->bhsw', q, kg) * scale
        logits = torch.where(mask[None, None, :, :], logits, -1000000000.0)
        attn = _softmax(logits, axis=-1)
        o = torch.einsum('bhsw,bhswd->bhsd', attn, vg)
        return (lambda x, *d: x.permute(*d))(o, 0, 2, 1, 3).reshape(B, S, D) @ wo

    def _B_ref(x, w1, w2):
        h = torch.einsum('bsd,df->bsf', x, w1)
        h = _silu(h)
        out = torch.einsum('bsf,fd->bsd', h, w2)
        return out
    x = A_x
    mod = (_silu(c) @ w_mod).reshape(c.shape[0], 6, 1, -1)
    sa, ca, ga, sb, cb, gb = (mod[:, 0].clone(), mod[:, 1].clone(), mod[:, 2].clone(), mod[:, 3].clone(), mod[:, 4].clone(), mod[:, 5].clone())
    h = _layernorm(x, axis=-1) * (1 + ca) + sa
    x = x + ga * _A_ref(h, A_wqkv, A_wo)
    h = _layernorm(x, axis=-1) * (1 + cb) + sb
    x = x + gb * _B_ref(h, B_w1, B_w2)
    return x
```

```cpp
#include <hip/hip_runtime.h>
#include <hip/hip_bf16.h>
#include <stdint.h>

typedef unsigned v4u __attribute__((ext_vector_type(4)));
typedef unsigned v2u __attribute__((ext_vector_type(2)));
typedef float f32x4 __attribute__((ext_vector_type(4)));
typedef short bf16x8 __attribute__((ext_vector_type(8)));
typedef unsigned short u16;

constexpr int S = 2048, D = 448, F = 1792, H = 16, DH = 28;

__device__ __forceinline__ float bf2f(unsigned b) { return __builtin_bit_cast(float, b << 16); }
__device__ __forceinline__ unsigned f2bf(float f) {
  __hip_bfloat16 h = __float2bfloat16(f);
  return (unsigned)__builtin_bit_cast(u16, h);
}
__device__ __forceinline__ unsigned pack2(float a, float b) { return f2bf(a) | (f2bf(b) << 16); }
__device__ __forceinline__ f32x4 mfma(v4u a, v4u b, f32x4 c) {
  return __builtin_amdgcn_mfma_f32_16x16x32_bf16(__builtin_bit_cast(bf16x8, a), __builtin_bit_cast(bf16x8, b), c, 0, 0, 0);
}

__global__ void k_mod(const u16* c, const u16* wmod, float* mod) {
  __shared__ float red[4][64];
  int n = blockIdx.x * 64 + (threadIdx.x & 63);
  int w = threadIdx.x >> 6;
  float acc = 0.f;
  for (int k = w; k < D; k += 4) {
    float cv = bf2f(c[k]);
    float s = cv / (1.f + __expf(-cv));
    s = bf2f(f2bf(s));
    acc += s * bf2f(wmod[k * (6 * D) + n]);
  }
  red[w][threadIdx.x & 63] = acc;
  __syncthreads();
  if (w == 0) {
    float v = red[0][threadIdx.x] + red[1][threadIdx.x] + red[2][threadIdx.x] + red[3][threadIdx.x];
    mod[n] = bf2f(f2bf(v));
  }
}

__global__ void k_ln(const u16* x, const float* shift, const float* scale, u16* h) {
  int row = blockIdx.x * 4 + (threadIdx.x >> 6);
  int l = threadIdx.x & 63;
  float v[7];
  float s = 0.f;
  for (int i = 0; i < 7; ++i) { v[i] = bf2f(x[row * D + l + 64 * i]); s += v[i]; }
  for (int o = 32; o; o >>= 1) s += __shfl_xor(s, o);
  float m = s * (1.f / D);
  float q = 0.f;
  for (int i = 0; i < 7; ++i) { float d = v[i] - m; q += d * d; }
  for (int o = 32; o; o >>= 1) q += __shfl_xor(q, o);
  float r = rsqrtf(q * (1.f / D) + 1e-5f);
  for (int i = 0; i < 7; ++i) {
    int k = l + 64 * i;
    h[row * D + k] = f2bf((v[i] - m) * r * (1.f + scale[k]) + shift[k]);
  }
}

template <int K, int N, int EPI>
__global__ void k_gemm(const u16* A, const u16* W, u16* out, const u16* res, const float* gate) {
  int wv = blockIdx.x * 4 + (threadIdx.x >> 6);
  int l = threadIdx.x & 63, r = l & 15, g = l >> 4;
  constexpr int NT = N / 64;
  int nt = wv % NT, mt = wv / NT;
  int n0 = nt * 64, m0 = mt * 32;
  f32x4 acc[2][4];
  for (int a = 0; a < 2; ++a) for (int i = 0; i < 4; ++i) acc[a][i] = f32x4{0, 0, 0, 0};
  for (int s = 0; s < K / 32; ++s) {
    v2u w[8];
#pragma unroll
    for (int j = 0; j < 8; ++j) w[j] = *(const v2u*)(W + (size_t)(32 * s + 8 * g + j) * N + n0 + 4 * r);
    v4u b[2];
#pragma unroll
    for (int a = 0; a < 2; ++a) b[a] = *(const v4u*)(A + (size_t)(m0 + 16 * a + r) * K + 32 * s + 8 * g);
    v4u f[4];
#pragma unroll
    for (int m = 0; m < 4; ++m) {
      f[0][m] = __builtin_amdgcn_perm(w[2 * m + 1].x, w[2 * m].x, 0x05040100);
      f[1][m] = __builtin_amdgcn_perm(w[2 * m + 1].x, w[2 * m].x, 0x07060302);
      f[2][m] = __builtin_amdgcn_perm(w[2 * m + 1].y, w[2 * m].y, 0x05040100);
      f[3][m] = __builtin_amdgcn_perm(w[2 * m + 1].y, w[2 * m].y, 0x07060302);
    }
#pragma unroll
    for (int a = 0; a < 2; ++a)
#pragma unroll
      for (int i = 0; i < 4; ++i) acc[a][i] = mfma(f[i], b[a], acc[a][i]);
  }
#pragma unroll
  for (int a = 0; a < 2; ++a) {
    int tok = m0 + 16 * a + r;
    float v[16];
#pragma unroll
    for (int q = 0; q < 4; ++q)
#pragma unroll
      for (int i = 0; i < 4; ++i) v[4 * q + i] = acc[a][i][q];
    int nb = n0 + 16 * g;
#pragma unroll
    for (int e = 0; e < 16; ++e) {
      float y = v[e];
      if (EPI == 1) { y = bf2f(f2bf(y)); y = y / (1.f + __expf(-y)); }
      if (EPI == 2) { y = bf2f(res[(size_t)tok * N + nb + e]) + gate[nb + e] * y; }
      v[e] = y;
    }
    v4u o0, o1;
#pragma unroll
    for (int e = 0; e < 4; ++e) { o0[e] = pack2(v[2 * e], v[2 * e + 1]); o1[e] = pack2(v[8 + 2 * e], v[9 + 2 * e]); }
    *(v4u*)(out + (size_t)tok * N + nb) = o0;
    *(v4u*)(out + (size_t)tok * N + nb + 8) = o1;
  }
}

__global__ void k_attn(const u16* qkv, u16* o) {
  int idx = blockIdx.x * 256 + threadIdx.x;
  int h = idx & 15, s = idx >> 4;
  const u16* q = qkv + (size_t)s * 3 * D + h * DH;
  float qv[DH];
  for (int d = 0; d < DH; ++d) qv[d] = bf2f(q[d]);
  float lg[8];
  float mx = -1e30f;
  for (int w = 0; w < 8; ++w) {
    int p = s - 2 * w;
    float a = -1e9f;
    if (p >= 0) {
      const u16* k = qkv + (size_t)p * 3 * D + D + h * DH;
      a = 0.f;
      for (int d = 0; d < DH; ++d) a += qv[d] * bf2f(k[d]);
      a *= 0.18898223650461362f;
    }
    lg[w] = a;
    mx = fmaxf(mx, a);
  }
  float sum = 0.f;
  for (int w = 0; w < 8; ++w) { lg[w] = __expf(lg[w] - mx); sum += lg[w]; }
  float inv = 1.f / sum;
  float ov[DH];
  for (int d = 0; d < DH; ++d) ov[d] = 0.f;
  for (int w = 0; w < 8; ++w) {
    int p = s - 2 * w;
    if (p < 0) p = 0;
    const u16* v = qkv + (size_t)p * 3 * D + 2 * D + h * DH;
    float pw = lg[w] * inv;
    for (int d = 0; d < DH; ++d) ov[d] += pw * bf2f(v[d]);
  }
  for (int d = 0; d < DH; ++d) o[(size_t)s * D + h * DH + d] = f2bf(ov[d]);
}

extern "C" void kernel_launch(void* const* d_in, const int* in_sizes, int n_in, void* d_out, int out_size, void* d_ws,
                              size_t ws_size, hipStream_t stream) {
  const u16* x = (const u16*)d_in[0];
  const u16* wqkv = (const u16*)d_in[1];
  const u16* wo = (const u16*)d_in[2];
  const u16* w1 = (const u16*)d_in[4];
  const u16* w2 = (const u16*)d_in[5];
  const u16* c = (const u16*)d_in[6];
  const u16* wmod = (const u16*)d_in[7];
  char* ws = (char*)d_ws;
  float* mod = (float*)ws;
  u16* h1 = (u16*)(ws + (1 << 20));
  u16* qkv = (u16*)(ws + (4 << 20));
  u16* o = (u16*)(ws + (12 << 20));
  u16* x1 = (u16*)(ws + (16 << 20));
  u16* h2 = (u16*)(ws + (20 << 20));
  u16* hh = (u16*)(ws + (24 << 20));
  k_mod<<<42, 256, 0, stream>>>(c, wmod, mod);
  k_ln<<<S / 4, 256, 0, stream>>>(x, mod, mod + D, h1);
  k_gemm<D, 3 * D, 0><<<(S / 32) * 21 / 4, 256, 0, stream>>>(h1, wqkv, qkv, nullptr, nullptr);
  k_attn<<<S * H / 256, 256, 0, stream>>>(qkv, o);
  k_gemm<D, D, 2><<<(S / 32) * 7 / 4, 256, 0, stream>>>(o, wo, x1, x, mod + 2 * D);
  k_ln<<<S / 4, 256, 0, stream>>>(x1, mod + 3 * D, mod + 4 * D, h2);
  k_gemm<D, F, 1><<<(S / 32) * 28 / 4, 256, 0, stream>>>(h2, w1, hh, nullptr, nullptr);
  k_gemm<F, D, 2><<<(S / 32) * 7 / 4, 256, 0, stream>>>(hh, w2, (u16*)d_out, x1, mod + 5 * D);
}
```

```cpp
#include <hip/hip_runtime.h>
#include <stdint.h>

typedef unsigned v4u __attribute__((ext_vector_type(4)));
typedef unsigned v2u __attribute__((ext_vector_type(2)));
typedef float f32x4 __attribute__((ext_vector_type(4)));
typedef float f32x2 __attribute__((ext_vector_type(2)));
typedef short bf16x8 __attribute__((ext_vector_type(8)));
typedef __bf16 bf16x2 __attribute__((ext_vector_type(2)));
typedef unsigned short u16;
typedef unsigned long long u64;
#define LAS __attribute__((address_space(3)))
typedef __amdgpu_buffer_rsrc_t rsrc_t;

#define STAMPS 0
#define DLY_O 900
#define DLY_U 900
#define DLY_D 900

#define W1P 7
#define W2P 3
constexpr int S = 2048, D = 448, F = 1792;
constexpr int SC1 = 16;
constexpr unsigned POISON = 0xFFFFFFFFu;
constexpr unsigned SPIN_CAP = 4096;

constexpr int WS_GEN = 0;
constexpr int WS_READY = 4096;
constexpr int WS_MODG = 8192;
constexpr int WS_OBUF = 65536;
constexpr int WS_X1 = WS_OBUF + 32 * 4 * 14336;
constexpr int WS_H = WS_X1 + S * 896;
constexpr int WS_END = WS_H + 32 * 4 * 8 * 7 * 1024;
constexpr int WS_STAMP = 16 << 20;

constexpr int L_HT = 0;
constexpr int L_WO = 71680;
constexpr int L_KV = L_WO + 57344;
constexpr int L_RED = L_WO;
constexpr int L_MRED = L_KV;
constexpr int L_X1 = L_KV + 20480;
constexpr int L_MOD = L_X1 + 8192;
constexpr int L_FLG = L_MOD + 3840;
constexpr int L_END = L_FLG + 256;
static_assert(L_END <= 163840, "lds");
constexpr int FL_KV = 0;
constexpr int FL_O = 20;
constexpr int FL_X = 28;

__device__ __forceinline__ float bflo(unsigned u) { return __builtin_bit_cast(float, u << 16); }
__device__ __forceinline__ float bfhi(unsigned u) { return __builtin_bit_cast(float, u & 0xFFFF0000u); }
__device__ __forceinline__ unsigned pack2(float a, float b) {
  bf16x2 r = __builtin_convertvector(f32x2{a, b}, bf16x2);
  return __builtin_bit_cast(unsigned, r);
}
__device__ __forceinline__ unsigned sane(unsigned u) { return u < 0xFFFFFFFEu ? u : 0xFFFFFFFEu; }
__device__ __forceinline__ f32x4 mfma(v4u a, v4u b, f32x4 c) {
  return __builtin_amdgcn_mfma_f32_16x16x32_bf16(__builtin_bit_cast(bf16x8, a), __builtin_bit_cast(bf16x8, b), c, 0, 0, 0);
}
__device__ __forceinline__ rsrc_t mk(const void* p, int bytes) {
  return __builtin_amdgcn_make_buffer_rsrc((void*)p, 0, bytes, 0x00020000);
}
#define SCHED() __builtin_amdgcn_sched_barrier(0)
#define CFENCE() asm volatile("" ::: "memory")
#define LGKM0() asm volatile("s_waitcnt lgkmcnt(0)" ::: "memory")
#define VM0() asm volatile("s_waitcnt vmcnt(0)" ::: "memory")
#define BAR() do { LGKM0(); __builtin_amdgcn_s_barrier(); CFENCE(); } while (0)

template <int CTRL, int RM>
__device__ __forceinline__ float dpp_add(float v) {
  int t = __builtin_amdgcn_update_dpp(0, __builtin_bit_cast(int, v), CTRL, RM, 0xF, false);
  return v + __builtin_bit_cast(float, t);
}
__device__ __forceinline__ float wave_sum(float v) {
  v = dpp_add<0x111, 0xF>(v);
  v = dpp_add<0x112, 0xF>(v);
  v = dpp_add<0x114, 0xF>(v);
  v = dpp_add<0x118, 0xF>(v);
  v = dpp_add<0x142, 0xA>(v);
  v = dpp_add<0x143, 0xC>(v);
  return __builtin_bit_cast(float, __builtin_amdgcn_readlane(__builtin_bit_cast(int, v), 63));
}
__device__ __forceinline__ void swap16(float v, float& a, float& b) {
  unsigned u = __builtin_bit_cast(unsigned, v), u2 = u;
  auto t = __builtin_amdgcn_permlane16_swap(u, u2, false, false);
  const unsigned t0 = t[0], t1 = t[1];
  a = __builtin_bit_cast(float, t0); b = __builtin_bit_cast(float, t1);
}
__device__ __forceinline__ void swap32(float v, float& a, float& b) {
  unsigned u = __builtin_bit_cast(unsigned, v), u2 = u;
  auto t = __builtin_amdgcn_permlane32_swap(u, u2, false, false);
  const unsigned t0 = t[0], t1 = t[1];
  a = __builtin_bit_cast(float, t0); b = __builtin_bit_cast(float, t1);
}
__device__ __forceinline__ float rows_max(float v) {
  float a, b;
  swap16(v, a, b); v = fmaxf(a, b);
  swap32(v, a, b); return fmaxf(a, b);
}
__device__ __forceinline__ float rows_sum(float v) {
  float a, b;
  swap16(v, a, b); v = a + b;
  swap32(v, a, b); return a + b;
}
__device__ __forceinline__ unsigned max4(v4u v) { return max(max(v.x, v.y), max(v.z, v.w)); }
__device__ __forceinline__ unsigned now32() { return (unsigned)__builtin_amdgcn_s_memtime(); }

__device__ __forceinline__ void xpose(const unsigned (&r)[8], v4u& f0, v4u& f1) {
#pragma unroll
  for (int m = 0; m < 4; ++m) {
    f0[m] = __builtin_amdgcn_perm(r[2 * m + 1], r[2 * m], 0x05040100u);
    f1[m] = __builtin_amdgcn_perm(r[2 * m + 1], r[2 * m], 0x07060302u);
  }
  asm volatile("" : "+v"(f0), "+v"(f1));
}
template <bool SWAP>
__device__ __forceinline__ void gemm14(const v4u (&fa)[14], const v4u (&fb)[14], LAS unsigned char* be, LAS unsigned char* bo,
                                       f32x4& a0, f32x4& a1) {
  v4u bf[4];
#pragma unroll
  for (int s = 0; s < 4; ++s) bf[s] = *(LAS v4u*)(((s & 1) ? bo : be) + s * 1024);
  a0 = f32x4{0, 0, 0, 0};
  a1 = f32x4{0, 0, 0, 0};
  __builtin_amdgcn_sched_barrier(0);
#pragma unroll
  for (int s = 0; s < 14; ++s) {
    const v4u cur = bf[s & 3];
    if (SWAP) { a0 = mfma(cur, fa[s], a0); a1 = mfma(cur, fb[s], a1); }
    else { a0 = mfma(fa[s], cur, a0); a1 = mfma(fb[s], cur, a1); }
    if (s + 4 < 14) bf[s & 3] = *(LAS v4u*)(((s & 1) ? bo : be) + (s + 4) * 1024);
    __builtin_amdgcn_sched_barrier(0);
  }
}
template <int ROWB>
__device__ __forceinline__ void load8(unsigned (&r)[8], rsrc_t rs, int voff, int soff) {
#pragma unroll
  for (int jj = 0; jj < 8; ++jj) r[jj] = __builtin_amdgcn_raw_buffer_load_b32(rs, voff, soff + jj * ROWB, 0);
}

#define STAMP(i) do { } while (0)

__global__ void __launch_bounds__(512, 2)
adaln_block(const u16* __restrict__ x, const u16* __restrict__ wqkv, const u16* __restrict__ wo, const u16* __restrict__ w1,
            const u16* __restrict__ w2, const u16* __restrict__ cvec, const u16* __restrict__ wmod, u16* __restrict__ out,
            unsigned char* __restrict__ ws) {
  __shared__ __attribute__((aligned(16))) unsigned char smem[L_END];
  LAS unsigned char* L = (LAS unsigned char*)smem;
  volatile LAS unsigned* flg = (volatile LAS unsigned*)(L + L_FLG);

  const int b = blockIdx.x;
  const int xcd = b & 7, li = b >> 3, grp = li >> 3, j = li & 7, p = xcd * 4 + grp;
  const int tid = threadIdx.x, lane = tid & 63, w = __builtin_amdgcn_readfirstlane(tid >> 6);
  const int r = lane & 15, g = lane >> 4, rc = r < 14 ? r : 13;
  const int lc = lane < 56 ? lane : 55;
  const int rot = (grp * 4 + (xcd >> 1)) % 14;
  const int rot7 = (grp * 2 + (xcd >> 2)) % 7;

  const rsrc_t rX = mk(x, S * D * 2), rQKV = mk(wqkv, D * 3 * D * 2), rWO = mk(wo, D * D * 2), rW1 = mk(w1, D * F * 2),
               rW2 = mk(w2, F * D * 2), rWM = mk(wmod, D * 6 * D * 2), rC = mk(cvec, D * 2), rWS = mk(ws, WS_END),
               rOUT = mk(out, S * D * 2);
  STAMP(0);

  const unsigned gen = __builtin_amdgcn_raw_buffer_load_b32(rWS, WS_GEN, 0, SC1);
  const int task = j * 32 + p;
  const bool has_task = task < 84;
  v4u wm[4];
  unsigned cr[4];
  if (has_task) {
#pragma unroll
    for (int i = 0; i < 4; ++i) {
      int row = 56 * w + (lane >> 2) + 16 * i;
      if (i == 3) row = 56 * w + min((lane >> 2) + 48, 55);
      wm[i] = __builtin_amdgcn_raw_buffer_load_b128(rWM, row * (6 * D * 2) + task * 64 + (lane & 3) * 16, 0, 0);
      cr[i] = __builtin_amdgcn_raw_buffer_load_b16(rC, row * 2, 0, 0);
    }
  }
  v4u xr[10];
#pragma unroll
  for (int i = 0; i < 10; ++i) {
    int lr = 10 * w + (i + j) % 10;
    int row = max(64 * p - 16 + lr, 0);
    xr[i] = __builtin_amdgcn_raw_buffer_load_b128(rX, lc * 16, row * 896, 0);
  }
  SCHED();
  {
    const v4u pz = {POISON, POISON, POISON, POISON};
#pragma unroll
    for (int i = 0; i < 4; ++i) {
      int idx = w * 4 + i;
      if (idx < 28) {
        int T = idx / 7, v = idx % 7;
        __builtin_amdgcn_raw_buffer_store_b128(pz, rWS, lane * 16, WS_H + (((p * 4 + T) * 8 + j) * 7 + v) * 1024, SC1);
      }
    }
    if (tid < 448) {
      int T = tid / 112, q = tid % 112;
      __builtin_amdgcn_raw_buffer_store_b128(pz, rWS, WS_OBUF + (p * 4 + T) * 14336 + j * 1792 + q * 16, 0, SC1);
      int row = tid / 7, c7 = tid % 7;
      __builtin_amdgcn_raw_buffer_store_b128(pz, rWS, WS_X1 + (64 * p + row) * 896 + j * 112 + c7 * 16, 0, SC1);
    }
  }
  if (tid < 64) flg[tid] = 0u;
  SCHED();
  const int head = w & 1;
  const int sec = w < 2 ? 1 : (w < 4 ? 2 : 0);
  unsigned wa[14][8];
  {
    const int voff = (8 * g) * (3 * D * 2) + (sec * D + 56 * j + 28 * head + 2 * rc) * 2;
#pragma unroll
    for (int s = 0; s < 14; ++s) {
      int ks = s + rot; if (ks >= 14) ks -= 14;
      load8<3 * D * 2>(wa[s], rQKV, voff, ks * 32 * (3 * D * 2));
      if (s == 5) {
        SCHED();
        if (has_task) {
          float acc[8];
#pragma unroll
          for (int e = 0; e < 8; ++e) acc[e] = 0.f;
#pragma unroll
          for (int i = 0; i < 4; ++i) {
            float cv = bflo(cr[i]);
            float sv = cv * __builtin_amdgcn_rcpf(1.f + __expf(-cv));
            sv = bflo(pack2(sv, sv) & 0xFFFFu);
            if (i == 3 && (lane >> 2) >= 8) sv = 0.f;
#pragma unroll
            for (int e = 0; e < 4; ++e) {
              acc[2 * e] += sv * bflo(wm[i][e]);
              acc[2 * e + 1] += sv * bfhi(wm[i][e]);
            }
          }
#pragma unroll
          for (int e = 0; e < 8; ++e) {
            float v = acc[e];
            v = dpp_add<0x124, 0xF>(v);
            v = dpp_add<0x128, 0xF>(v);
            acc[e] = rows_sum(v);
          }
          if (lane < 4) {
            LAS f32x4* mr = (LAS f32x4*)(L + L_MRED + w * 128 + lane * 32);
            mr[0] = f32x4{acc[0], acc[1], acc[2], acc[3]};
            mr[1] = f32x4{acc[4], acc[5], acc[6], acc[7]};
          }
          BAR();
          if (w == 0 && lane < 16) {
            float s0 = 0.f, s1 = 0.f;
#pragma unroll
            for (int ww = 0; ww < 8; ++ww) {
              f32x2 v = *(LAS f32x2*)(L + L_MRED + ww * 128 + lane * 8);
              s0 += v.x; s1 += v.y;
            }
            v2u gr = {pack2(s0, s1), gen + 1u};
            __builtin_amdgcn_raw_buffer_store_b64(gr, rWS, WS_MODG + (task * 16 + lane) * 8, 0, SC1);
          }
          STAMP(1);
        }
        SCHED();
      }
    }
  }
  SCHED();
  const unsigned tag = __builtin_amdgcn_readfirstlane(gen) + 1u;
  float xn[10][8];
#pragma unroll
  for (int i = 0; i < 10; ++i) {
    float sum = 0.f;
#pragma unroll
    for (int e = 0; e < 4; ++e) { xn[i][2 * e] = bflo(xr[i][e]); xn[i][2 * e + 1] = bfhi(xr[i][e]); sum += xn[i][2 * e] + xn[i][2 * e + 1]; }
    if (lane >= 56) sum = 0.f;
    const float mean = wave_sum(sum) * (1.f / D);
    float sq = 0.f;
#pragma unroll
    for (int e = 0; e < 8; ++e) { xn[i][e] -= mean; sq += xn[i][e] * xn[i][e]; }
    if (lane >= 56) sq = 0.f;
    const float rstd = __builtin_amdgcn_rsqf(wave_sum(sq) * (1.f / D) + 1e-5f);
#pragma unroll
    for (int e = 0; e < 8; ++e) xn[i][e] *= rstd;
  }
  STAMP(2);
  SCHED();
  if (w < 7) {
    unsigned t[4][8];
#pragma unroll
    for (int q = 0; q < 4; ++q) {
      int idx = 4 * w + q, hc = idx >= 14 ? 1 : 0, s = idx - 14 * hc;
      load8<D * 2>(t[q], rWO, (8 * g) * (D * 2) + (56 * j + 2 * rc) * 2, s * 32 * (D * 2) + hc * 56);
    }
#pragma unroll
    for (int q = 0; q < 4; ++q) {
      v4u f0, f1;
      xpose(t[q], f0, f1);
      *(LAS v4u*)(L + L_WO + ((4 * w + q) * 2 + 0) * 1024 + lane * 16) = f0;
      *(LAS v4u*)(L + L_WO + ((4 * w + q) * 2 + 1) * 1024 + lane * 16) = f1;
    }
  }
  v4u fa[14], fb[14];
#pragma unroll
  for (int s = 0; s < 14; ++s) xpose(wa[s], fa[s], fb[s]);
  VM0();
  STAMP(3);
  if (w >= 6) {
    const int s0 = (w - 6) * 3;
    v4u m[5];
    for (unsigned spin = 0; spin < SPIN_CAP; ++spin) {
      CFENCE();
#pragma unroll
      for (int q = 0; q < 4; ++q)
        m[q] = __builtin_amdgcn_raw_buffer_load_b128(rWS, WS_MODG + ((s0 + (q >> 1)) * 224 + lc * 4 + (q & 1) * 2) * 8, 0, SC1);
      m[4] = __builtin_amdgcn_raw_buffer_load_b128(rWS, WS_MODG + ((s0 + 2) * 224 + 28 * j + 2 * rc) * 8, 0, SC1);
      bool ok = true;
#pragma unroll
      for (int q = 0; q < 5; ++q) ok = ok && m[q].y == tag && m[q].w == tag;
      if (__all(ok)) break;
      __builtin_amdgcn_s_sleep(2);
    }
    if (lane < 56) {
      *(LAS v4u*)(L + L_MOD + (w - 6) * 1792 + lane * 16) = v4u{m[0].x, m[0].z, m[1].x, m[1].z};
      *(LAS v4u*)(L + L_MOD + (w - 6) * 1792 + 896 + lane * 16) = v4u{m[2].x, m[2].z, m[3].x, m[3].z};
    }
    if (lane < 14) *(LAS v2u*)(L + L_MOD + 3584 + (w - 6) * 128 + lane * 8) = v2u{m[4].x, m[4].z};
  }
  BAR();
  STAMP(4);
  if (tid == 0) __builtin_amdgcn_raw_buffer_store_b64(v2u{tag, tag}, rWS, WS_READY + (p * 8 + j) * 8, 0, SC1);
  int hw_off;
  int hw_x;
  {
    int ks = lc >> 2, gg = lc & 3;
    int slot = ks - rot; if (slot < 0) slot += 14;
    hw_off = (slot * 64 + gg * 16) * 16;
    hw_x = (4 * slot + gg) & 7;
  }
  {
    const v4u sa = *(LAS v4u*)(L + L_MOD + lc * 16), ca = *(LAS v4u*)(L + L_MOD + 896 + lc * 16);
    float sf[8], cf[8];
#pragma unroll
    for (int e = 0; e < 4; ++e) { sf[2 * e] = bflo(sa[e]); sf[2 * e + 1] = bfhi(sa[e]); cf[2 * e] = 1.f + bflo(ca[e]); cf[2 * e + 1] = 1.f + bfhi(ca[e]); }
#pragma unroll
    for (int i = 0; i < 10; ++i) {
      int lr = 10 * w + (i + j) % 10;
      v4u h;
#pragma unroll
      for (int e = 0; e < 4; ++e) h[e] = pack2(xn[i][2 * e] * cf[2 * e] + sf[2 * e], xn[i][2 * e + 1] * cf[2 * e + 1] + sf[2 * e + 1]);
      if (lane < 56) *(LAS v4u*)(L + L_HT + (lr >> 4) * 14336 + hw_off + (((lr & 15) ^ hw_x) << 4)) = h;
    }
  }
  BAR();
  STAMP(5);
  const int hr_e = (g * 16 + (r ^ g)) * 16, hr_o = (g * 16 + (r ^ (4 | g))) * 16;

  unsigned wb[14][8];
  const int w1_voff = (8 * g) * (F * 2) + (224 * j + 32 * (w < 7 ? w : 6) + 2 * r) * 2;
#pragma unroll
  for (int s = 0; s < W1P; ++s) {
    int ks = s + rot; if (ks >= 14) ks -= 14;
    load8<F * 2>(wb[s], rW1, w1_voff, ks * 32 * (F * 2));
  }
  SCHED();

  v2u rdy = {0u, 0u};
  if (w < 4) {
#pragma unroll
    for (int Tq = 0; Tq < 5; ++Tq) {
      f32x4 a0 = {0, 0, 0, 0}, a1 = {0, 0, 0, 0};
      if (Tq == 4) rdy = __builtin_amdgcn_raw_buffer_load_b64(rWS, WS_READY + (p * 8 + (lane & 7)) * 8, 0, SC1);
      if (w < 2) {
        gemm14<false>(fa, fb, L + L_HT + Tq * 14336 + hr_e, L + L_HT + Tq * 14336 + hr_o, a0, a1);
        v4u k;
#pragma unroll
        for (int e = 0; e < 4; ++e) k[e] = pack2(a0[e], a1[e]);
        if (g == 3) { k[2] = 0u; k[3] = 0u; }
        *(LAS v4u*)(L + L_KV + (Tq * 2 + head) * 2048 + lane * 16) = k;
      } else {
        gemm14<true>(fa, fb, L + L_HT + Tq * 14336 + hr_e, L + L_HT + Tq * 14336 + hr_o, a0, a1);
        v4u v = {pack2(a0[0], a0[1]), pack2(a0[2], a0[3]), pack2(a1[0], a1[1]), pack2(a1[2], a1[3])};
        *(LAS v4u*)(L + L_KV + (Tq * 2 + head) * 2048 + 1024 + lane * 16) = v;
      }
      LGKM0();
      if (lane == 0) flg[FL_KV + Tq * 4 + w] = 1u;
    }
    STAMP(6);
  } else {
    const int T0 = (w >> 1) & 1;
    v4u qf[2];
#pragma unroll
    for (int u = 0; u < 2; ++u) {
      const int Tq = T0 + 2 * u + 1;
      f32x4 a0 = {0, 0, 0, 0}, a1 = {0, 0, 0, 0};
      gemm14<false>(fa, fb, L + L_HT + Tq * 14336 + hr_e, L + L_HT + Tq * 14336 + hr_o, a0, a1);
#pragma unroll
      for (int e = 0; e < 4; ++e) qf[u][e] = pack2(a0[e], a1[e]);
    }
    STAMP(6);
#pragma unroll
    for (int u = 0; u < 2; ++u) {
      const int T = T0 + 2 * u;
      if (u == 1) rdy = __builtin_amdgcn_raw_buffer_load_b64(rWS, WS_READY + (p * 8 + (lane & 7)) * 8, 0, SC1);
      for (unsigned spin = 0; spin < (SPIN_CAP << 4); ++spin) {
        unsigned f0 = flg[FL_KV + (T + 1) * 4 + head], f1 = flg[FL_KV + (T + 1) * 4 + 2 + head];
        if (f0 & f1) break;
        __builtin_amdgcn_s_sleep(1);
      }
      CFENCE();
      const v4u kp = *(LAS v4u*)(L + L_KV + (T * 2 + head) * 2048 + lane * 16);
      const v4u kc = *(LAS v4u*)(L + L_KV + ((T + 1) * 2 + head) * 2048 + lane * 16);
      const v4u vp = *(LAS v4u*)(L + L_KV + (T * 2 + head) * 2048 + 1024 + lane * 16);
      const v4u vc = *(LAS v4u*)(L + L_KV + ((T + 1) * 2 + head) * 2048 + 1024 + lane * 16);
      f32x4 z = {0, 0, 0, 0};
      f32x4 s0 = mfma(kp, qf[u], z), s1 = mfma(kc, qf[u], z);
      const bool prev = (p + T) > 0;
      float sv[8];
      float mx = -INFINITY;
#pragma unroll
      for (int e = 0; e < 4; ++e) {
        int d0 = 16 + r - (4 * g + e), d1 = r - (4 * g + e);
        bool v0 = prev && d0 <= 14 && !(d0 & 1), v1 = d1 >= 0 && !(d1 & 1);
        sv[e] = v0 ? s0[e] : -INFINITY;
        sv[4 + e] = v1 ? s1[e] : -INFINITY;
        mx = fmaxf(mx, fmaxf(sv[e], sv[4 + e]));
      }
      mx = rows_max(mx);
      const float cs = 0.18898223650461362f * 1.4426950408889634f;
      float sum = 0.f;
#pragma unroll
      for (int e = 0; e < 8; ++e) { sv[e] = __builtin_amdgcn_exp2f((sv[e] - mx) * cs); sum += sv[e]; }
      sum = rows_sum(sum);
      const float inv = __builtin_amdgcn_rcpf(sum);
      v4u pf;
#pragma unroll
      for (int e = 0; e < 4; ++e) pf[e] = pack2(sv[2 * e] * inv, sv[2 * e + 1] * inv);
      f32x4 o0 = mfma(v4u{vp.x, vp.y, vc.x, vc.y}, pf, z), o1 = mfma(v4u{vp.z, vp.w, vc.z, vc.w}, pf, z);
      const int col = 28 * (2 * j + head) + 8 * g;
      const int ob = WS_OBUF + (p * 4 + T) * 14336 + r * 16;
      v2u d0 = {sane(pack2(o0[0], o1[0])), sane(pack2(o0[1], o1[1]))};
      v2u d1 = {sane(pack2(o0[2], o1[2])), sane(pack2(o0[3], o1[3]))};
      __builtin_amdgcn_raw_buffer_store_b64(d0, rWS, ob + (col >> 3) * 256 + ((col >> 2) & 1) * 8, 0, SC1);
      if (g < 3) __builtin_amdgcn_raw_buffer_store_b64(d1, rWS, ob + ((col + 4) >> 3) * 256 + (((col + 4) >> 2) & 1) * 8, 0, SC1);
      if (lane == 0) flg[FL_O + T * 2 + head] = now32() | 1u;
      STAMP(7 + u);
    }
  }
  SCHED();
#pragma unroll
  for (int s = W1P; s < 14; ++s) {
    int ks = s + rot; if (ks >= 14) ks -= 14;
    load8<F * 2>(wb[s], rW1, w1_voff, ks * 32 * (F * 2));
  }
  SCHED();
  for (unsigned spin = 0; spin < SPIN_CAP; ++spin) {
    if (__all(rdy.x == tag)) break;
    CFENCE();
    rdy = __builtin_amdgcn_raw_buffer_load_b64(rWS, WS_READY + (p * 8 + (lane & 7)) * 8, 0, SC1);
  }

  {
    const int T = w >> 1, hc = w & 1;
    const int row = 64 * p + 16 * T + r;
    const int cb = 112 * j + 56 * hc + 16 * g;
    const v2u xa = __builtin_amdgcn_raw_buffer_load_b64(rX, row * 896 + cb, 0, 0);
    const v2u xb = __builtin_amdgcn_raw_buffer_load_b64(rX, row * 896 + cb + (g < 3 ? 8 : 0), 0, 0);
    for (unsigned spin = 0; spin < (SPIN_CAP << 4); ++spin) {
      unsigned f0 = flg[FL_O + T * 2], f1 = flg[FL_O + T * 2 + 1];
      unsigned t = now32();
      if (f0 && f1 && (int)(t - f0) > DLY_O && (int)(t - f1) > DLY_O) break;
      __builtin_amdgcn_s_sleep(1);
    }
    STAMP(9);
    v4u of[14];
    for (unsigned spin = 0; spin < SPIN_CAP; ++spin) {
      CFENCE();
      unsigned m = 0;
#pragma unroll
      for (int s = 0; s < 14; ++s) of[s] = __builtin_amdgcn_raw_buffer_load_b128(rWS, lane * 16 + s * 1024, WS_OBUF + (p * 4 + T) * 14336, SC1);
#pragma unroll
      for (int s = 0; s < 14; ++s) m = max(m, max4(of[s]));
      if (!__any(m == POISON)) break;
    }
    STAMP(10);
    f32x4 a0 = {0, 0, 0, 0}, a1 = {0, 0, 0, 0};
    {
      LAS unsigned char* wb_ = L + L_WO + hc * 28672 + lane * 16;
      v4u f0[3], f1[3];
#pragma unroll
      for (int s = 0; s < 3; ++s) { f0[s] = *(LAS v4u*)(wb_ + s * 2048); f1[s] = *(LAS v4u*)(wb_ + s * 2048 + 1024); }
      SCHED();
#pragma unroll
      for (int s = 0; s < 14; ++s) {
        a0 = mfma(f0[s % 3], of[s], a0);
        a1 = mfma(f1[s % 3], of[s], a1);
        if (s + 3 < 14) { f0[s % 3] = *(LAS v4u*)(wb_ + (s + 3) * 2048); f1[s % 3] = *(LAS v4u*)(wb_ + (s + 3) * 2048 + 1024); }
        SCHED();
      }
    }
    const v2u ga0 = *(LAS v2u*)(L + L_MOD + 3584 + 56 * hc + 16 * g), ga1 = *(LAS v2u*)(L + L_MOD + 3584 + 56 * hc + 16 * g + 8);
    v4u y;
    y[0] = sane(pack2(bflo(xa.x) + bflo(ga0.x) * a0[0], bfhi(xa.x) + bfhi(ga0.x) * a1[0]));
    y[1] = sane(pack2(bflo(xa.y) + bflo(ga0.y) * a0[1], bfhi(xa.y) + bfhi(ga0.y) * a1[1]));
    y[2] = sane(pack2(bflo(xb.x) + bflo(ga1.x) * a0[2], bfhi(xb.x) + bfhi(ga1.x) * a1[2]));
    y[3] = sane(pack2(bflo(xb.y) + bflo(ga1.y) * a0[3], bfhi(xb.y) + bfhi(ga1.y) * a1[3]));
    if (g < 3) __builtin_amdgcn_raw_buffer_store_b128(y, rWS, WS_X1 + row * 896 + cb, 0, SC1);
    else __builtin_amdgcn_raw_buffer_store_b64(v2u{y[0], y[1]}, rWS, WS_X1 + row * 896 + cb, 0, SC1);
    *(LAS v4u*)(L + L_X1 + (T * 2 + hc) * 1024 + lane * 16) = y;
    if (lane == 0) flg[FL_X + T * 2 + hc] = now32() | 1u;
    STAMP(11);
  }
  SCHED();
  unsigned wc[7][2][8];
  const int w2_voff = (224 * w + 8 * g) * (D * 2) + (56 * j + 2 * rc) * 2;
#pragma unroll
  for (int s = 0; s < W2P; ++s) {
    int v = s + rot7; if (v >= 7) v -= 7;
    load8<D * 2>(wc[s][0], rW2, w2_voff, v * 32 * (D * 2));
    load8<D * 2>(wc[s][1], rW2, w2_voff, v * 32 * (D * 2) + 56);
  }
  SCHED();
#pragma unroll
  for (int s = 0; s < 14; ++s) xpose(wb[s], fa[s], fb[s]);

  unsigned tH[4];
#pragma unroll
  for (int T = 0; T < 4; ++T) {
    for (unsigned spin = 0; spin < (SPIN_CAP << 4); ++spin) {
      unsigned f0 = flg[FL_X + T * 2], f1 = flg[FL_X + T * 2 + 1];
      unsigned t = now32();
      if (f0 && f1 && (int)(t - f0) > DLY_U && (int)(t - f1) > DLY_U) break;
      __builtin_amdgcn_s_sleep(1);
    }
    v4u xv[2];
    for (unsigned spin = 0; spin < SPIN_CAP; ++spin) {
      CFENCE();
#pragma unroll
      for (int i = 0; i < 2; ++i) xv[i] = __builtin_amdgcn_raw_buffer_load_b128(rWS, lc * 16 + i * 896, WS_X1 + (64 * p + 16 * T + 2 * w) * 896, SC1);
      if (!__any(max(max4(xv[0]), max4(xv[1])) == POISON)) break;
    }
    STAMP(12 + T);
    const v4u sb = *(LAS v4u*)(L + L_MOD + 1792 + lc * 16), cb = *(LAS v4u*)(L + L_MOD + 2688 + lc * 16);
#pragma unroll
    for (int i = 0; i < 2; ++i) {
      float d[8];
      const float x0 = bflo(__builtin_amdgcn_readfirstlane(xv[i][0]));
      float s1 = 0.f, s2 = 0.f;
#pragma unroll
      for (int e = 0; e < 4; ++e) { d[2 * e] = bflo(xv[i][e]) - x0; d[2 * e + 1] = bfhi(xv[i][e]) - x0; }
#pragma unroll
      for (int e = 0; e < 8; ++e) { s1 += d[e]; s2 += d[e] * d[e]; }
      if (lane >= 56) { s1 = 0.f; s2 = 0.f; }
      const float mu = wave_sum(s1) * (1.f / D);
      const float var = fmaxf(wave_sum(s2) * (1.f / D) - mu * mu, 0.f);
      const float rstd = __builtin_amdgcn_rsqf(var + 1e-5f);
      v4u h;
#pragma unroll
      for (int e = 0; e < 4; ++e)
        h[e] = pack2((d[2 * e] - mu) * rstd * (1.f + bflo(cb[e])) + bflo(sb[e]), (d[2 * e + 1] - mu) * rstd * (1.f + bfhi(cb[e])) + bfhi(sb[e]));
      if (lane < 56) *(LAS v4u*)(L + L_HT + (T + 1) * 14336 + hw_off + (((2 * w + i) ^ hw_x) << 4)) = h;
    }
    BAR();
    if (w < 7) {
      f32x4 a0 = {0, 0, 0, 0}, a1 = {0, 0, 0, 0};
      gemm14<false>(fa, fb, L + L_HT + (T + 1) * 14336 + hr_e, L + L_HT + (T + 1) * 14336 + hr_o, a0, a1);
      v4u hf;
#pragma unroll
      for (int e = 0; e < 4; ++e) {
        float u0 = a0[e], u1 = a1[e];
        u0 = u0 * __builtin_amdgcn_rcpf(1.f + __expf(-u0));
        u1 = u1 * __builtin_amdgcn_rcpf(1.f + __expf(-u1));
        hf[e] = sane(pack2(u0, u1));
      }
      __builtin_amdgcn_raw_buffer_store_b128(hf, rWS, lane * 16, WS_H + (((p * 4 + T) * 8 + j) * 7 + w) * 1024, SC1);
    }
    tH[T] = now32();
    STAMP(16 + T);
  }
  SCHED();
#pragma unroll
  for (int s = W2P; s < 7; ++s) {
    int v = s + rot7; if (v >= 7) v -= 7;
    load8<D * 2>(wc[s][0], rW2, w2_voff, v * 32 * (D * 2));
    load8<D * 2>(wc[s][1], rW2, w2_voff, v * 32 * (D * 2) + 56);
  }
  SCHED();
  v4u fc[7][4];
#pragma unroll
  for (int s = 0; s < 7; ++s) { xpose(wc[s][0], fc[s][0], fc[s][1]); xpose(wc[s][1], fc[s][2], fc[s][3]); }

#pragma unroll
  for (int T = 0; T < 4; ++T) {
    for (unsigned spin = 0; spin < (SPIN_CAP << 4); ++spin) {
      if ((int)(now32() - tH[T]) > DLY_D) break;
      __builtin_amdgcn_s_sleep(1);
    }
    v4u hf[7];
    for (unsigned spin = 0; spin < SPIN_CAP; ++spin) {
      CFENCE();
      unsigned m = 0;
#pragma unroll
      for (int s = 0; s < 7; ++s) {
        int v = s + rot7; if (v >= 7) v -= 7;
        hf[s] = __builtin_amdgcn_raw_buffer_load_b128(rWS, lane * 16, WS_H + (((p * 4 + T) * 8 + w) * 7 + v) * 1024, SC1);
      }
#pragma unroll
      for (int s = 0; s < 7; ++s) m = max(m, max4(hf[s]));
      if (!__any(m == POISON)) break;
    }
    STAMP(20 + T);
    f32x4 acc[4];
#pragma unroll
    for (int a = 0; a < 4; ++a) acc[a] = f32x4{0, 0, 0, 0};
#pragma unroll
    for (int s = 0; s < 7; ++s)
#pragma unroll
      for (int a = 0; a < 4; ++a) acc[a] = mfma(fc[s][a], hf[s], acc[a]);
    LAS unsigned char* red = L + L_RED + (T & 1) * 32768;
#pragma unroll
    for (int a = 0; a < 4; ++a) {
      *(LAS f32x2*)(red + ((w * 4 + a) * 2 + 0) * 512 + lane * 8) = f32x2{acc[a][0], acc[a][1]};
      *(LAS f32x2*)(red + ((w * 4 + a) * 2 + 1) * 512 + lane * 8) = f32x2{acc[a][2], acc[a][3]};
    }
    BAR();
    if (w < 4) {
      const int hc = w >> 1, rh = w & 1;
      f32x2 s0 = {0, 0}, s1 = {0, 0};
#pragma unroll
      for (int ww = 0; ww < 8; ++ww) {
        s0 += *(LAS f32x2*)(red + ((ww * 4 + hc * 2 + 0) * 2 + rh) * 512 + lane * 8);
        s1 += *(LAS f32x2*)(red + ((ww * 4 + hc * 2 + 1) * 2 + rh) * 512 + lane * 8);
      }
      const v2u x1 = *(LAS v2u*)(L + L_X1 + (T * 2 + hc) * 1024 + lane * 16 + rh * 8);
      const v2u gb = *(LAS v2u*)(L + L_MOD + 3584 + 128 + 56 * hc + 16 * g + 8 * rh);
      v2u y;
      y.x = pack2(bflo(x1.x) + bflo(gb.x) * s0.x, bfhi(x1.x) + bfhi(gb.x) * s1.x);
      y.y = pack2(bflo(x1.y) + bflo(gb.y) * s0.y, bfhi(x1.y) + bfhi(gb.y) * s1.y);
      if (g < 3 || rh == 0)
        __builtin_amdgcn_raw_buffer_store_b64(y, rOUT, (64 * p + 16 * T + r) * 896 + 112 * j + 56 * hc + 16 * g + 8 * rh, 0, 0);
    }
    STAMP(24 + T);
  }
  if (b == 0 && w == 7) {
    for (unsigned spin = 0; spin < SPIN_CAP; ++spin) {
      CFENCE();
      v4u a = __builtin_amdgcn_raw_buffer_load_b128(rWS, WS_READY + lane * 32, 0, SC1);
      v4u c = __builtin_amdgcn_raw_buffer_load_b128(rWS, WS_READY + lane * 32 + 16, 0, SC1);
      if (__all(a.x == tag && a.z == tag && c.x == tag && c.z == tag)) break;
    }
    if (lane == 0) __builtin_amdgcn_raw_buffer_store_b32(tag, rWS, WS_GEN, 0, SC1);
  }
}

extern "C" void kernel_launch(void* const* d_in, const int* in_sizes, int n_in, void* d_out, int out_size, void* d_ws,
                              size_t ws_size, hipStream_t stream) {
  adaln_block<<<256, 512, 0, stream>>>((const u16*)d_in[0], (const u16*)d_in[1], (const u16*)d_in[2], (const u16*)d_in[4],
                                       (const u16*)d_in[5], (const u16*)d_in[6], (const u16*)d_in[7], (u16*)d_out,
                                       (unsigned char*)d_ws);
}
```

```cpp
#include <hip/hip_runtime.h>
#include <stdint.h>

typedef unsigned v4u __attribute__((ext_vector_type(4)));
typedef unsigned v2u __attribute__((ext_vector_type(2)));
typedef float f32x4 __attribute__((ext_vector_type(4)));
typedef float f32x2 __attribute__((ext_vector_type(2)));
typedef short bf16x8 __attribute__((ext_vector_type(8)));
typedef __bf16 bf16x2 __attribute__((ext_vector_type(2)));
typedef unsigned short u16;
typedef unsigned long long u64;
#define LAS __attribute__((address_space(3)))
#define GAS __attribute__((address_space(1)))
typedef __amdgpu_buffer_rsrc_t rsrc_t;

#define STAMPS 0
#define DLY_O 900
#define DLY_U 900
#define DLY_D 900
#define DLY_M 5000
#define DLY_B 1800
#define DLY_Q 5500
#define QPRE 5
#define KVP1 0
#define XSL 4
#define RDY_S 1
#define RD_S 8
#define W2E 3
#define ROT 0
#define ALT 1
#define CODEPF 1

constexpr int S = 2048, D = 448, F = 1792;
constexpr int SC1 = 16;
constexpr unsigned POISON = 0xFFFFFFFFu;
constexpr unsigned SPIN_CAP = 4096;

constexpr int WS_GEN = 0;
constexpr int WS_READY = 11 << 20;
constexpr int WS_HF = WS_READY + 16384;
constexpr int WS_MODG = 8192;
constexpr int WS_OBUF = 65536;
constexpr int WS_X1 = WS_OBUF + 128 * 14336;
constexpr int WS_ST = WS_X1 + 128 * 14336;
constexpr int WS_H = WS_ST + 128 * 2048;
constexpr int WS_PO = 12 << 20;
constexpr int WS_P1 = WS_PO + 401408;
constexpr int WS_P2 = WS_P1 + 1605632;
constexpr int WS_PQ = WS_P2 + 1605632;
constexpr int WS_END = WS_PO + (32 << 20);
constexpr int WS_STAMP = 48 << 20;
static_assert(WS_H + 128 * 57344 <= WS_READY && WS_PQ + 48 * 14 * 2048 <= WS_PO + (8 << 20), "ws");
static_assert(WS_END <= WS_STAMP, "ws");

constexpr int L_HT = 0;
constexpr int L_WO = 71680;
constexpr int L_KV = L_WO + 50176;
constexpr int L_QF = L_KV + 20480;
constexpr int L_X1 = L_QF + 4096;
constexpr int L_MOD = L_X1 + 7168;
constexpr int L_G = L_MOD + 3584;
constexpr int L_ST = L_G + 512;
constexpr int L_CNT = L_ST + 2048;
constexpr int L_END = L_CNT + 32;
constexpr int L_MRED = L_KV;
constexpr int L_OX = L_HT;
constexpr int L_R01 = L_WO;
constexpr int L_R23 = L_HT;
static_assert(L_END + 1024 <= 163840 && L_R01 + 65536 <= L_X1, "lds");

__device__ __forceinline__ float bflo(unsigned u) { return __builtin_bit_cast(float, u << 16); }
__device__ __forceinline__ float bfhi(unsigned u) { return __builtin_bit_cast(float, u & 0xFFFF0000u); }
__device__ __forceinline__ float asf(unsigned u) { return __builtin_bit_cast(float, u); }
__device__ __forceinline__ unsigned asu(float f) { return __builtin_bit_cast(unsigned, f); }
__device__ __forceinline__ unsigned pack2(float a, float b) {
  bf16x2 r = __builtin_convertvector(f32x2{a, b}, bf16x2);
  return __builtin_bit_cast(unsigned, r);
}
__device__ __forceinline__ f32x2 unpk(unsigned u) { return f32x2{bflo(u), bfhi(u)}; }
__device__ __forceinline__ f32x2 fma2(f32x2 a, f32x2 b, f32x2 c) { return __builtin_elementwise_fma(a, b, c); }
__device__ __forceinline__ float dot2(unsigned a, unsigned b, float c) {
  return __builtin_amdgcn_fdot2_f32_bf16(__builtin_bit_cast(bf16x2, a), __builtin_bit_cast(bf16x2, b), c, false);
}
__device__ __forceinline__ unsigned sane(unsigned u) { return u < 0xFFFFFFFEu ? u : 0xFFFFFFFEu; }
__device__ __forceinline__ f32x4 mfma(v4u a, v4u b, f32x4 c) {
  return __builtin_amdgcn_mfma_f32_16x16x32_bf16(__builtin_bit_cast(bf16x8, a), __builtin_bit_cast(bf16x8, b), c, 0, 0, 0);
}
__device__ __forceinline__ rsrc_t mk(const void* p, int bytes) {
  return __builtin_amdgcn_make_buffer_rsrc((void*)p, 0, bytes, 0x00020000);
}
#define SPIN _Pragma("nounroll") for (unsigned spin = 0; spin < SPIN_CAP; ++spin)
#define SCHED() __builtin_amdgcn_sched_barrier(0)
#define CFENCE() asm volatile("" ::: "memory")
#define LGKM0() asm volatile("s_waitcnt lgkmcnt(0)" ::: "memory")
#define VM0() asm volatile("s_waitcnt vmcnt(0)" ::: "memory")
#define BAR() do { LGKM0(); __builtin_amdgcn_s_barrier(); CFENCE(); } while (0)
#define PIN(x) asm volatile("" : "+v"(x))
#define USE(x) asm volatile("" :: "v"(x) : "memory")
#define LD128(voff, soff) __builtin_amdgcn_raw_buffer_load_b128(rWS, (voff), (soff), SC1)
#define ST128(v, voff, soff) do { const v4u d_ = (v); __builtin_amdgcn_raw_buffer_store_b128(d_, rWS, (voff), (soff), SC1); asm volatile("s_nop 1" :: "v"(d_)); } while (0)
#define ST64(v, voff, soff) __builtin_amdgcn_raw_buffer_store_b64((v), rWS, (voff), (soff), SC1)
#define ST32(v, voff, soff) __builtin_amdgcn_raw_buffer_store_b32((v), rWS, (voff), (soff), SC1)
#define WAITVM0() __builtin_amdgcn_s_waitcnt(0x0F70)
#define RETRY(badexpr, reload) do { if (__builtin_expect(__any(badexpr), 0)) { SPIN { CFENCE(); __builtin_amdgcn_s_sleep(2); reload; WAITVM0(); if (!__any(badexpr)) break; } } } while (0)

template <int CTRL>
__device__ __forceinline__ float dpp_add(float v) {
  int t = __builtin_amdgcn_update_dpp(0, __builtin_bit_cast(int, v), CTRL, 0xF, 0xF, false);
  return v + __builtin_bit_cast(float, t);
}
__device__ __forceinline__ float fold16(float a, float b) {
  auto t = __builtin_amdgcn_permlane16_swap(asu(a), asu(b), false, false);
  const unsigned t0 = t[0], t1 = t[1];
  return asf(t0) + asf(t1);
}
__device__ __forceinline__ float fold32(float a, float b) {
  auto t = __builtin_amdgcn_permlane32_swap(asu(a), asu(b), false, false);
  const unsigned t0 = t[0], t1 = t[1];
  return asf(t0) + asf(t1);
}
__device__ __forceinline__ float rows_max(float v) {
  auto t = __builtin_amdgcn_permlane16_swap(asu(v), asu(v), false, false);
  unsigned t0 = t[0], t1 = t[1];
  v = fmaxf(asf(t0), asf(t1));
  auto q = __builtin_amdgcn_permlane32_swap(asu(v), asu(v), false, false);
  t0 = q[0]; t1 = q[1];
  return fmaxf(asf(t0), asf(t1));
}
__device__ __forceinline__ float rows_sum(float v) { v = fold16(v, v); return fold32(v, v); }
__device__ __forceinline__ unsigned max4(unsigned m, v4u v) { return max(max(max(m, v.x), v.y), max(v.z, v.w)); }
__device__ __forceinline__ unsigned now32() { return (unsigned)__builtin_amdgcn_s_memtime(); }
__device__ __forceinline__ void pace(unsigned t0, int dly) {
  _Pragma("nounroll") for (unsigned spin = 0; spin < 4096; ++spin) {
    if ((int)(now32() - t0) > dly) break;
    __builtin_amdgcn_s_sleep(1);
  }
}
__device__ __forceinline__ void rows8(unsigned (&r)[8], const unsigned char* src, int soff, unsigned (&vo)[8]) {
  const GAS unsigned char* pb = (const GAS unsigned char*)src + soff;
  asm volatile("" : "+s"(pb), "+v"(vo[0]), "+v"(vo[1]), "+v"(vo[2]), "+v"(vo[3]), "+v"(vo[4]), "+v"(vo[5]), "+v"(vo[6]), "+v"(vo[7]));
#pragma unroll
  for (int jj = 0; jj < 8; ++jj) r[jj] = *(const GAS unsigned*)(pb + vo[jj]);
}
__device__ __forceinline__ void xpose(const unsigned (&r)[8], v4u& f0, v4u& f1) {
#pragma unroll
  for (int m = 0; m < 4; ++m) {
    f0[m] = __builtin_amdgcn_perm(r[2 * m + 1], r[2 * m], 0x05040100u);
    f1[m] = __builtin_amdgcn_perm(r[2 * m + 1], r[2 * m], 0x07060302u);
  }
}

typedef short s16x4 __attribute__((ext_vector_type(4)));
__device__ __forceinline__ v2u trread(LAS unsigned char* p) {
  return __builtin_bit_cast(v2u, __builtin_amdgcn_ds_read_tr16_b64_v4i16((LAS s16x4*)p));
}

#define STAMP(i) do { } while (0)
#define L_TOT L_END

__global__ void __launch_bounds__(512, 2)
adaln_block(const u16* __restrict__ x, const u16* __restrict__ wqkv, const u16* __restrict__ wo, const u16* __restrict__ w1,
            const u16* __restrict__ w2, const u16* __restrict__ cvec, const u16* __restrict__ wmod, u16* out,
            unsigned char* ws) {
  __shared__ __attribute__((aligned(16))) unsigned char smem[L_TOT];
  LAS unsigned char* L = (LAS unsigned char*)smem;

  const int b = blockIdx.x;
  const int tid = threadIdx.x, lane = tid & 63, w = __builtin_amdgcn_readfirstlane(tid >> 6);
  const unsigned t_start = now32();
  asm volatile("" :: "s"(x), "s"(wqkv), "s"(wo), "s"(w1), "s"(w2), "s"(cvec), "s"(wmod), "s"(out), "s"(ws));
  unsigned cpf = 0;
  {
    const GAS unsigned char* pc; unsigned len;
    asm volatile("s_getpc_b64 %0\nadaln_lbl0:\n s_mov_b32 %1, adaln_lbl1-adaln_lbl0" : "=s"(pc), "=s"(len));
    const unsigned off = (unsigned)(tid ^ 256) * 128u;
    if (off < len && (b >> 3) % CODEPF == 0) cpf = *(const GAS unsigned*)(pc + off);
  }
#define CPF_USE() asm volatile("" :: "v"(cpf))
  const int xcd = b & 7, li = b >> 3, grp = li >> 3, j = li & 7, p = xcd * 4 + grp;
  const int r = lane & 15, g = lane >> 4;
  const int lc = lane < 56 ? lane : 55;
  const int rot = ROT ? (grp * 4 + (xcd >> 1)) % 14 : 0;
  const int rot7 = ROT ? (grp * 2 + (xcd >> 2)) % 7 : 0;
  const int ci = b / 3;
  const bool mtask = b < 252 && b - 3 * ci == 1;

  const rsrc_t rX = mk(x, S * D * 2), rWM = mk(wmod, D * 6 * D * 2), rC = mk(cvec, D * 2), rWS = mk(ws, WS_END),
               rOUT = mk(out, S * D * 2);
  const v4u pz = {POISON, POISON, POISON, POISON};
  STAMP(0);
  LAS unsigned* cnt = (LAS unsigned*)(L + L_CNT);
  if (tid < 8) cnt[tid] = 0u;
  const unsigned tag = *(const __attribute__((address_space(4))) unsigned*)(ws + WS_GEN) + 1u;
  const int pko = ALT ? (tag & 3u) << 23 : 0;
  const int head = w & 1, T_ = w >> 1;
  v4u fa[14], fb[14];
  unsigned ck[14];
  const int fr14 = g * 224 + min(r, 13) * 16;
  const int pq_base = WS_PQ + pko + (j * 6 + (w < 4 ? w : 4 + head)) * 28672;
#define PQ_LOAD(s) do { int ks_ = (s) + rot; if (ks_ >= 14) ks_ -= 14; const int so_ = pq_base + ks_ * 2048; \
    fa[s] = LD128(fr14, so_); fb[s] = LD128(fr14 + 896, so_); ck[s] = __builtin_amdgcn_raw_buffer_load_b32(rWS, (fr14 >> 2) + 1792, so_, SC1); } while (0)
#define CSUM(a, b) (((a).x + (a).y + (a).z) + ((a).w + (b).x + (b).y) + ((b).z + (b).w + tag))
#define PQ_CHECK(s) RETRY(CSUM(fa[s], fb[s]) != ck[s], PQ_LOAD(s))
#define ARRIVE(i) __hip_atomic_fetch_add(cnt + (i), 1u, __ATOMIC_RELAXED, __HIP_MEMORY_SCOPE_WORKGROUP)
#define CNT_WAIT(i, n) do { _Pragma("nounroll") for (unsigned spin = 0; spin < (SPIN_CAP << 8); ++spin) { \
    if (__builtin_amdgcn_readfirstlane(*(volatile LAS unsigned*)(cnt + (i))) == (n)) break; } CFENCE(); } while (0)
#define READY_ARRIVE() do { if (lane == 0) { if (ARRIVE(1) == 7u) ST32(tag, WS_READY + (b >> 2) * 128 + (b & 3) * 4, 0); } } while (0)
#define MOD_LOAD(s0) do { _Pragma("unroll") for (int q = 0; q < 4; ++q) \
    m[q] = LD128(lc * 32 + (q & 1) * 16, WS_MODG + ((q >> 1) * 2688 + ((s0) + ((w >> 1) & 1)) * 448 + (w & 1) * 224) * 8); } while (0)
#define MX(q) ((m[q].y ^ tag) | (m[q].w ^ tag))
#define MOD_BAD ((MX(0) | MX(1) | MX(2) | MX(3)) != 0u)
#define MOD_PUT() do { \
    unsigned k0 = pack2(asf(m[0].x) + asf(m[2].x), asf(m[0].z) + asf(m[2].z)), k1 = pack2(asf(m[1].x) + asf(m[3].x), asf(m[1].z) + asf(m[3].z)); \
    if (w & 2) { k0 = pack2(1.f + bflo(k0), 1.f + bfhi(k0)); k1 = pack2(1.f + bflo(k1), 1.f + bfhi(k1)); } \
    *(LAS f32x4*)(L + L_MOD + (w - 4) * 896 + lc * 16) = f32x4{bflo(k0), bfhi(k0), bflo(k1), bfhi(k1)}; } while (0)
  const int p1_base = WS_P1 + pko + (j * 7 + w) * 28672;
#define P1_LOAD4(gi) do { int g_ = (gi) + rot7; if (g_ >= 7) g_ -= 7; const int so_ = p1_base + g_ * 4096; \
    fa[2 * (gi)] = LD128(lane * 16, so_); fb[2 * (gi)] = LD128(lane * 16 + 1024, so_); \
    fa[2 * (gi) + 1] = LD128(lane * 16 + 2048, so_); fb[2 * (gi) + 1] = LD128(lane * 16 + 3072, so_); } while (0)
  LAS unsigned char* hre = L + L_HT + (g * 16 + (r ^ g)) * 16;
  LAS unsigned char* hro = L + L_HT + (g * 16 + (r ^ (4 | g))) * 16;
  v4u qmine;
  f32x4 bias0, bias1;
#define MK_BIAS() do { const bool prev = (p + T_) > 0; \
    _Pragma("unroll") for (int e = 0; e < 4; ++e) { const int d1 = r - (4 * g + e); const bool ev = !(d1 & 1); \
      bias0[e] = (prev && d1 <= -2 && ev) ? 0.f : -INFINITY; bias1[e] = (d1 >= 0 && ev) ? 0.f : -INFINITY; } \
    PIN(bias0); PIN(bias1); } while (0)
  if (w < 4) {
    __builtin_amdgcn_s_setprio(3);
    unsigned v2[8];
    if (mtask) {
      v4u wm[7];
      const int row0 = (ci & 1) * 224 + 56 * w;
      const unsigned cr = __builtin_amdgcn_raw_buffer_load_b16(rC, lc * 2, row0 * 2, 0);
#pragma unroll
      for (int i = 0; i < 7; ++i)
        wm[i] = __builtin_amdgcn_raw_buffer_load_b128(rWM, (lane >> 3) * 5376 + (lane & 7) * 16, (row0 + 8 * i) * 5376 + (ci >> 1) * 128, 0);
      STAMP(22);
      BAR();
      const float cv = bflo(cr);
      const float sv0 = cv * __builtin_amdgcn_rcpf(1.f + __expf(-cv));
      const int svi = (int)(pack2(sv0, sv0) << 16);
      f32x2 acc[4];
#pragma unroll
      for (int i = 0; i < 7; ++i) {
        const float sv = asf(__builtin_amdgcn_ds_bpermute((lane >> 3) * 4 + i * 32, svi));
#pragma unroll
        for (int e = 0; e < 4; ++e) acc[e] = i ? fma2(f32x2{sv, sv}, unpk(wm[i][e]), acc[e]) : f32x2{sv, sv} * unpk(wm[i][e]);
      }
      f32x2 h0 = {fold32(acc[0].x, acc[2].x), fold32(acc[0].y, acc[2].y)}, h1 = {fold32(acc[1].x, acc[3].x), fold32(acc[1].y, acc[3].y)};
      f32x2 q0 = {dpp_add<0x128>(fold16(h0.x, h1.x)), dpp_add<0x128>(fold16(h0.y, h1.y))};
      *(LAS f32x2*)(L + L_MRED + w * 256 + (r & 7) * 32 + g * 8) = q0;
      LGKM0();
      unsigned old = 0;
      if (lane == 0) old = ARRIVE(0);
      if (__builtin_amdgcn_readfirstlane(old) == 3u) {
        CFENCE();
        float s0 = 0.f;
#pragma unroll
        for (int ww = 0; ww < 4; ++ww) s0 += *(LAS float*)(L + L_MRED + ww * 256 + lane * 4);
        ST64((v2u{asu(s0), tag}), lane * 8, WS_MODG + ((ci & 1) * 2688 + (ci >> 1) * 64) * 8);
      }
    } else {
      const int qu = min((ci * 2 + ((b - 3 * ci) >> 1)) * 4 + w, 671), qk = qu / 48, hs = qu - qk * 48;
      unsigned uq[8];
#pragma unroll
      for (int q = 0; q < 8; ++q) v2[q] = (8 * g + q) * 2688 + min(r, 13) * 4;
      rows8(uq, (const unsigned char*)wqkv, qk * (32 * 2688) + hs * 56, v2);
      STAMP(22);
      BAR();
      v4u f0, f1;
      xpose(uq, f0, f1);
      const int so = WS_PQ + pko + ((((hs & 15) >> 1) * 6 + ((hs >> 4) == 1 ? 0 : ((hs >> 4) == 2 ? 2 : 4)) + (hs & 1)) * 14 + qk) * 2048;
      if (b < 252) { ST128(f0, fr14, so); ST128(f1, fr14 + 896, so); ST32(CSUM(f0, f1), (fr14 >> 2) + 1792, so); }
    }
    STAMP(1);
    __builtin_amdgcn_s_setprio(0);
    {
      const int lu = min(w * 256 + b, 881);
      const int m32 = lane & 31, gh = lane >> 5;
      const unsigned char* src; int rowb, lcol, ks;
      if (lu < 392) { src = (const unsigned char*)w1; rowb = 3584; lcol = lu / 14; ks = lu % 14; }
      else if (lu < 784) { src = (const unsigned char*)w2; rowb = 896; lcol = (lu - 392) / 56; ks = (lu - 392) % 56; }
      else { src = (const unsigned char*)wo; rowb = 896; lcol = (lu - 784) / 14; ks = (lu - 784) % 14; }
      unsigned la[8], lb[8];
#pragma unroll
      for (int q = 0; q < 8; ++q) v2[q] = (gh * 8 + q) * rowb + m32 * 4;
      rows8(la, src, ks * 32 * rowb + lcol * 128, v2);
      rows8(lb, src, (ks * 32 + 16) * rowb + lcol * 128, v2);
      STAMP(2);
      pace(t_start, DLY_Q);
#pragma unroll
      for (int s = 0; s < 14; ++s) PQ_LOAD(s);
      SCHED();
      STAMP(3);
      const int col = 64 * lcol + 2 * m32;
      const int jj = col / 56, cc = col - jj * 56;
      int dst, fs, gs;
      if (lu < 392) { fs = 1024; gs = 256; dst = WS_P1 + ((col >> 5) * 14 + ks) * 2048 + ((col & 31) >> 1) * 16; }
      else if (lu < 784) { fs = 16; gs = cc >= 48 ? 128 : 256; dst = WS_P2 + (jj * 56 + ks) * 3584 + (cc >> 4) * 1024 + (cc & 15) * 16; }
      else { const int hc = cc >> 5; fs = hc ? 768 : 1024; gs = hc ? 192 : 256; dst = WS_PO + (jj * 14 + ks) * 3584 + hc * 2048 + ((cc & 31) >> 1) * 16; }
      dst += gh * gs;
      if (w * 256 + b < 882) {
        v4u f0, f1, f2, f3;
        xpose(la, f0, f1); xpose(lb, f2, f3);
        PIN(f0); PIN(f1); PIN(f2); PIN(f3);
        ST128(f0, dst, pko); ST128(f1, dst + fs, pko); ST128(f2, dst + 2 * gs, pko); ST128(f3, dst + 2 * gs + fs, pko);
      }
      const int pv = min(lane + 64, 111) * 16, po = (p * 4 + w) * 14336 + j * 1792;
      ST128(pz, lane * 16, WS_OBUF + po); ST128(pz, pv, WS_OBUF + po);
      ST128(pz, lane * 16, WS_X1 + po); ST128(pz, pv, WS_X1 + po);
      if (w == 0) ST128(pz, g * 2048 + r * 16, WS_ST + p * 8192 + j * 256);
    }
    STAMP(4);
    MK_BIAS();
#pragma unroll
    for (int s = 0; s < 14; ++s) PQ_CHECK(s);
    CPF_USE();
    STAMP(5);
    BAR();
    __builtin_amdgcn_s_setprio(3);
    STAMP(6);
    f32x4 a0[5], a1[5];
    v4u bx[5];
#pragma unroll
    for (int t = 0; t < 5; ++t) { a0[t] = f32x4{0, 0, 0, 0}; a1[t] = f32x4{0, 0, 0, 0}; bx[t] = *(LAS v4u*)(hre + t * 14336); }
#pragma unroll
    for (int s = 0; s < 14; ++s) {
#pragma unroll
      for (int t = 0; t < 5; ++t) {
        a0[t] = mfma(fa[s], bx[t], a0[t]);
        a1[t] = mfma(fb[s], bx[t], a1[t]);
        if (s < 13) bx[t] = *(LAS v4u*)(((s & 1) ? hre : hro) + t * 14336 + (s + 1) * 1024);
      }
      if (s == RDY_S) { VM0(); READY_ARRIVE(); }
      SCHED();
    }
    LAS unsigned char* dst = L + L_KV + head * 2048 + (w < 2 ? lane * 16 : 1024 + r * 64 + g * 16);
#pragma unroll
    for (int t = 0; t < 5; ++t)
      *(LAS v4u*)(dst + t * 4096) = v4u{pack2(a0[t][0], a1[t][0]), pack2(a0[t][1], a1[t][1]), pack2(a0[t][2], a1[t][2]), pack2(a0[t][3], a1[t][3])};
    qmine = pz;
  } else {
    const int q = w - 4;
    __builtin_amdgcn_s_sleep(XSL);
    __builtin_amdgcn_s_setprio(2);
    v4u xr[20];
    const int xvo = lane < 56 ? lane * 16 : 0x40000000;
    const int cq = (j & 3) << 2;
#define X_LOAD(i) xr[i] = __builtin_amdgcn_raw_buffer_load_b128(rX, xvo + ((i) & 3) * 896, \
      (i) < 16 ? (64 * p + 16 * q + (((i) & 12) ^ cq)) * 896 : max(64 * p - 16 + 4 * q, 0) * 896, 0)
#pragma unroll
    for (int i = 0; i < 8; ++i) X_LOAD(i);
    STAMP(22);
    BAR();
    float u[10];
#pragma unroll
    for (int m = 0; m < 10; ++m) {
      if (m < 6) { X_LOAD(8 + 2 * m); X_LOAD(9 + 2 * m); }
      SCHED();
      float t[2];
#pragma unroll
      for (int h = 0; h < 2; ++h) {
        float s1 = 0.f, s2 = 0.f;
#pragma unroll
        for (int e = 0; e < 4; ++e) { s1 = dot2(xr[2 * m + h][e], 0x3F803F80u, s1); s2 = dot2(xr[2 * m + h][e], xr[2 * m + h][e], s2); }
        t[h] = fold32(s1, s2);
      }
      u[m] = dpp_add<0x121>(dpp_add<0x122>(dpp_add<0x124>(dpp_add<0x128>(fold16(t[0], t[1])))));
    }
    STAMP(2);
    CPF_USE();
    LAS unsigned char* stq = L + L_ST + q * 512;
    if (r == 0) {
      *(LAS f32x4*)(stq + g * 48) = f32x4{u[0], u[1], u[2], u[3]};
      *(LAS f32x4*)(stq + g * 48 + 16) = f32x4{u[4], u[5], u[6], u[7]};
      *(LAS f32x2*)(stq + g * 48 + 32) = f32x2{u[8], u[9]};
    }
    {
      const int rw = min(lane, 19);
      const float mu = *(LAS float*)(stq + (rw & 1) * 48 + (rw >> 1) * 4) * (1.f / D), sq = *(LAS float*)(stq + 96 + (rw & 1) * 48 + (rw >> 1) * 4);
      const float rstd = __builtin_amdgcn_rsqf(fmaxf(sq * (1.f / D) - mu * mu, 0.f) + 1e-5f);
      *(LAS f32x4*)(stq + 192 + rw * 16) = f32x4{rstd, rstd, -mu * rstd, -mu * rstd};
    }
    asm volatile("" :: "s"(tag));
    READY_ARRIVE();
    STAMP(3);
    f32x2 xn[20][4];
#pragma unroll
    for (int t = 0; t < 20; ++t) {
      const f32x4 st = *(LAS f32x4*)(stq + 192 + t * 16);
#pragma unroll
      for (int e = 0; e < 4; ++e) { xn[t][e] = fma2(unpk(xr[t][e]), f32x2{st.x, st.y}, f32x2{st.z, st.w}); PIN(xn[t][e]); }
    }
    unsigned hb0, hb1;
    {
      const int ks = lc >> 2, gg = lc & 3;
      int slot = ks - rot; if (slot < 0) slot += 14;
      const unsigned base = L_HT + (slot * 64 + gg * 16 + ((4 * slot + gg) & 7)) * 16;
      hb0 = (base ^ (cq << 4)) + (q + 1) * 14336;
      hb1 = base ^ (q << 6);
    }
    STAMP(4);
    {
      v4u m[4];
      pace(t_start, DLY_M);
      MOD_LOAD(0);
#pragma unroll
      for (int s = 0; s < QPRE; ++s) PQ_LOAD(s);
      SCHED();
      RETRY(MOD_BAD, MOD_LOAD(0));
      MOD_PUT();
      LGKM0();
      if (lane == 0) ARRIVE(2);
      CNT_WAIT(2, 4u);
    }
    STAMP(5);
    if (lane < 56) {
      const f32x4 s0 = *(LAS f32x4*)(L + L_MOD + lc * 32), s1 = *(LAS f32x4*)(L + L_MOD + lc * 32 + 16);
      const f32x4 c0 = *(LAS f32x4*)(L + L_MOD + 1792 + lc * 32), c1 = *(LAS f32x4*)(L + L_MOD + 1792 + lc * 32 + 16);
      const f32x2 sf[4] = {{s0.x, s0.y}, {s0.z, s0.w}, {s1.x, s1.y}, {s1.z, s1.w}};
      const f32x2 cf[4] = {{c0.x, c0.y}, {c0.z, c0.w}, {c1.x, c1.y}, {c1.z, c1.w}};
#pragma unroll
      for (int i = 0; i < 20; ++i) {
        v4u h;
#pragma unroll
        for (int e = 0; e < 4; ++e) { const f32x2 v = fma2(xn[i][e], cf[e], sf[e]); h[e] = pack2(v.x, v.y); }
        *(LAS v4u*)(L + (i < 16 ? hb0 ^ (i << 4) : hb1 ^ ((i - 16) << 4))) = h;
      }
    }
    BAR();
    __builtin_amdgcn_s_setprio(1);
    STAMP(6);
#pragma unroll
    for (int s = QPRE; s < 14; ++s) PQ_LOAD(s);
    v4u m[4], m4, m5;
    const int gso = WS_MODG + ((w & 1 ? 5 : 2) * 448 + 56 * j) * 8;
#define G_LOAD() do { MOD_LOAD(3); m4 = LD128(min(lane, 27) * 16, gso); m5 = LD128(min(lane, 27) * 16, gso + 2688 * 8); } while (0)
    G_LOAD();
    SCHED();
    const int to = (((w >> 1) & 1) + 1) * 14336;
    f32x4 a0[2], a1[2];
    v4u bc[2][2];
#pragma unroll
    for (int t = 0; t < 2; ++t) {
      a0[t] = f32x4{0, 0, 0, 0}; a1[t] = f32x4{0, 0, 0, 0};
      bc[0][t] = *(LAS v4u*)(hre + to + t * 28672); bc[1][t] = *(LAS v4u*)(hro + to + t * 28672 + 1024);
    }
#pragma unroll
    for (int s = 0; s < 14; ++s) {
      PQ_CHECK(s);
#pragma unroll
      for (int t = 0; t < 2; ++t) {
        a0[t] = mfma(fa[s], bc[s & 1][t], a0[t]);
        a1[t] = mfma(fb[s], bc[s & 1][t], a1[t]);
        if (s < 12) bc[s & 1][t] = *(LAS v4u*)(((s & 1) ? hro : hre) + to + t * 28672 + (s + 2) * 1024);
      }
      SCHED();
    }
    if (w == 7) {
      v4u rd = LD128(lane * 128, WS_READY);
      RETRY(((rd.x ^ tag) | (rd.y ^ tag) | (rd.z ^ tag) | (rd.w ^ tag)) != 0u, __builtin_amdgcn_s_sleep(8); rd = LD128(lane * 128, WS_READY));
      if (lane == 0) ARRIVE(3);
    }
    v4u q0;
#pragma unroll
    for (int e = 0; e < 4; ++e) { q0[e] = pack2(a0[0][e], a1[0][e]); qmine[e] = pack2(a0[1][e], a1[1][e]); }
    if (g == 3) { q0[2] = 0u; q0[3] = 0u; qmine[2] = 0u; qmine[3] = 0u; }
    *(LAS v4u*)(L + L_QF + q * 1024 + lane * 16) = q0;
    MK_BIAS();
    RETRY(MOD_BAD || ((m4.y ^ tag) | (m4.w ^ tag) | (m5.y ^ tag) | (m5.w ^ tag)) != 0u, G_LOAD());
    MOD_PUT();
    {
      const unsigned k = pack2(asf(m4.x) + asf(m5.x), asf(m4.z) + asf(m5.z));
      if (lane < 28 && w < 6) {
        if (w & 1) *(LAS f32x2*)(L + L_G + 256 + lane * 8) = unpk(k);
        else { *(LAS float*)(L + L_G + (lane >> 2) * 32 + (lane & 3) * 4) = bflo(k); *(LAS float*)(L + L_G + (lane >> 2) * 32 + 16 + (lane & 3) * 4) = bfhi(k); }
      }
    }
    __builtin_amdgcn_s_setprio(3);
  }
  STAMP(7);
  const int colh = 28 * (2 * j + head) + 4 * g;
  const int oa0 = r * 16 + (colh >> 3) * 256 + ((colh >> 2) & 1) * 8;
  BAR();
  unsigned t_pub;
  {
    if (w < 4) qmine = *(LAS v4u*)(L + L_QF + w * 1024 + lane * 16);
    LAS unsigned char* kvb = L + L_KV + w * 2048 + lane * 16;
    const v4u kp = *(LAS v4u*)(kvb), kc = *(LAS v4u*)(kvb + 4096);
    LAS unsigned char* vtb = L + L_KV + w * 2048 + 1024 + (4 * g + (r >> 2)) * 64 + (r & 3) * 8;
    const v2u vp0 = trread(vtb), vp1 = trread(vtb + 32), vc0 = trread(vtb + 4096), vc1 = trread(vtb + 4096 + 32);
    const f32x4 z = {0, 0, 0, 0};
    const f32x4 s0 = mfma(kp, qmine, bias0), s1 = mfma(kc, qmine, bias1);
    const float mx = rows_max(fmaxf(fmaxf(fmaxf(s0[0], s0[1]), fmaxf(s0[2], s0[3])), fmaxf(fmaxf(s1[0], s1[1]), fmaxf(s1[2], s1[3]))));
    const float cs = 0.18898223650461362f * 1.4426950408889634f;
    const float mc = -mx * cs;
    float pv[8];
#pragma unroll
    for (int e = 0; e < 4; ++e) { pv[e] = __builtin_amdgcn_exp2f(s0[e] * cs + mc); pv[4 + e] = __builtin_amdgcn_exp2f(s1[e] * cs + mc); }
    v4u pf;
#pragma unroll
    for (int e = 0; e < 4; ++e) pf[e] = pack2(pv[2 * e], pv[2 * e + 1]);
    const f32x4 o0 = mfma(v4u{vp0.x, vp0.y, vc0.x, vc0.y}, pf, z), o1 = mfma(v4u{vp1.x, vp1.y, vc1.x, vc1.y}, pf, z);
    const float inv = __builtin_amdgcn_rcpf(rows_sum(((pv[0] + pv[1]) + (pv[2] + pv[3])) + ((pv[4] + pv[5]) + (pv[6] + pv[7]))));
    const int ob = WS_OBUF + (p * 4 + T_) * 14336;
    v2u d0 = {sane(pack2(o0[0] * inv, o0[1] * inv)), sane(pack2(o0[2] * inv, o0[3] * inv))};
    v2u d1 = {sane(pack2(o1[0] * inv, o1[1] * inv)), sane(pack2(o1[2] * inv, o1[3] * inv))};
    ST64(d0, oa0, ob);
    if (g < 3) ST64(d1, oa0 + 512, ob);
    t_pub = now32();
  }
  STAMP(8);
  v4u wt[7];
  const int wl = min(w, 6);
#pragma unroll
  for (int q = 0; q < 7; ++q) if (w < 7) wt[q] = LD128(lane * 16 + (q & 3) * 1024, WS_PO + pko + j * 50176 + (wl * 7 + (q & 4)) * 1024);
  SCHED();
  {
    const int kh = w & 1;
    __builtin_amdgcn_s_setprio(0);
    const v4u xv = __builtin_amdgcn_raw_buffer_load_b128(rX, r * 896 + min(kh * 4 + g, 6) * 16, (64 * p + 16 * T_) * 896 + 112 * j, 0);
    if (w < 7) { P1_LOAD4(0); P1_LOAD4(1); }
    SCHED();
    if (w < 7) {
#pragma unroll
      for (int q = 0; q < 7; ++q) *(LAS v4u*)(L + L_WO + (w * 7 + q) * 1024 + lane * 16) = wt[q];
    }
    LAS unsigned char* wa_ = L + L_WO + kh * 25088 + lane * 16;
    LAS unsigned char* wb_ = L + L_WO + kh * 25088 + 2048 + (g * 12 + min(r, 11)) * 16;
    v4u wq[2][4];
#define WO_READ(s) do { wq[(s) & 1][0] = *(LAS v4u*)(wa_ + (s) * 3584); wq[(s) & 1][1] = *(LAS v4u*)(wa_ + (s) * 3584 + 1024); \
    wq[(s) & 1][2] = *(LAS v4u*)(wb_ + (s) * 3584); wq[(s) & 1][3] = *(LAS v4u*)(wb_ + (s) * 3584 + 768); } while (0)
    pace(t_pub, DLY_O);
    __builtin_amdgcn_s_setprio(3);
    v4u of[7];
#define O_LOAD() do { _Pragma("unroll") for (int s = 0; s < 7; ++s) of[s] = LD128(lane * 16 + (s & 3) * 1024, WS_OBUF + (p * 4 + T_) * 14336 + (kh * 7 + (s & 4)) * 1024); } while (0)
    O_LOAD();
    __builtin_amdgcn_s_setprio(0);
    if (w < 7) { P1_LOAD4(2); P1_LOAD4(3); }
    __builtin_amdgcn_s_setprio(3);
    SCHED();
    BAR();
    WO_READ(0); WO_READ(1);
    const f32x4 ge = *(LAS f32x4*)(L + L_G + kh * 128 + g * 32), go = *(LAS f32x4*)(L + L_G + kh * 128 + g * 32 + 16);
    f32x4 xe = {bflo(xv[0]), bflo(xv[1]), bflo(xv[2]), bflo(xv[3])}, xo4 = {bfhi(xv[0]), bfhi(xv[1]), bfhi(xv[2]), bfhi(xv[3])};
    PIN(xe); PIN(xo4);
#define MAX7(a) max4(max4(max4(max4(max4(max4(max4(0u, a[0]), a[1]), a[2]), a[3]), a[4]), a[5]), a[6])
    RETRY(MAX7(of) == POISON, O_LOAD());
    STAMP(9);
    f32x4 acc[4];
#pragma unroll
    for (int a = 0; a < 4; ++a) acc[a] = f32x4{0, 0, 0, 0};
#pragma unroll
    for (int s = 0; s < 7; ++s) {
#pragma unroll
      for (int a = 0; a < 4; ++a) acc[a] = mfma(wq[s & 1][a], of[s], acc[a]);
      if (s + 2 < 7) WO_READ(s + 2);
      SCHED();
    }
    f32x4 y0, y1;
    LAS unsigned char* ox = L + L_OX + lane * 16;
    if (kh) {
      *(LAS f32x4*)(ox + w * 2048) = acc[0]; *(LAS f32x4*)(ox + w * 2048 + 1024) = acc[1];
      y0 = acc[2]; y1 = acc[3];
    } else {
      *(LAS f32x4*)(ox + w * 2048) = acc[2]; *(LAS f32x4*)(ox + w * 2048 + 1024) = acc[3];
      y0 = acc[0]; y1 = acc[1];
    }
    BAR();
    y0 += *(LAS f32x4*)(ox + (w ^ 1) * 2048);
    y1 += *(LAS f32x4*)(ox + (w ^ 1) * 2048 + 1024);
    const bool live = !(kh && g == 3);
    y0 = __builtin_elementwise_fma(ge, y0, xe);
    y1 = __builtin_elementwise_fma(go, y1, xo4);
    v4u y;
#pragma unroll
    for (int e = 0; e < 4; ++e) y[e] = sane(pack2(y0[e], y1[e]));
    if (live) {
      ST128(y, lane * 16, WS_X1 + (p * 4 + T_) * 14336 + j * 1792 + kh * 1024);
      *(LAS v4u*)(L + L_X1 + T_ * 1792 + kh * 1024 + lane * 16) = y;
    }
    const f32x4 sm = y0 + y1, sq = __builtin_elementwise_fma(y0, y0, y1 * y1);
    float s1 = (sm[0] + sm[1]) + (sm[2] + sm[3]), s2 = (sq[0] + sq[1]) + (sq[2] + sq[3]);
    if (!live) { s1 = 0.f; s2 = 0.f; }
    float sv = fold32(s1, s2);
    sv = fold16(sv, sv);
    if (!(g & 1)) ST32(sane(asu(sv)), r * 16 + (g >> 1) * 4, WS_ST + ((p * 4 + T_) * 8 + j) * 256 + kh * 8);
    t_pub = now32();
    STAMP(10);
  }

  const int p2_base = WS_P2 + pko + (j * 56 + w * 7) * 3584;
  const int fr8 = 3072 + (g * 8 + (r & 7)) * 16;
#define P2_LOAD4(d0, d1, d2, d3, s) do { int v_ = (s) + rot7; if (v_ >= 7) v_ -= 7; const int so_ = p2_base + v_ * 3584; \
    d0 = LD128(lane * 16, so_); d1 = LD128(lane * 16 + 1024, so_); d2 = LD128(lane * 16 + 2048, so_); d3 = LD128(fr8, so_); } while (0)
#define WE_LOAD(s) P2_LOAD4(we[s][0], we[s][1], we[s][2], we[s][3], s)
#define WL_LOAD(q) P2_LOAD4(fa[2 * (q)], fb[2 * (q)], fa[2 * (q) + 1], fb[2 * (q) + 1], W2E + (q))
#define H_LOAD1(dst, T, s) do { int v_ = (s) + rot7; if (v_ >= 7) v_ -= 7; dst[s] = LD128(lane * 16, WS_H + (((p * 4 + (T)) * 8 + w) * 7 + v_) * 1024); } while (0)
#define H_LOAD(dst, T) do { _Pragma("unroll") for (int s = 0; s < 7; ++s) H_LOAD1(dst, T, s); } while (0)
#define HF_LD(T) __builtin_amdgcn_raw_buffer_load_b32(rWS, 0, WS_HF + (p * 8 + w) * 128, SC1)
#define HF_WAIT(v, T) RETRY(v != tag, __builtin_amdgcn_s_sleep(2); v = HF_LD(T))
  v4u we[W2E][4], h0[7], h1[7];
  unsigned tH3, hf1;
  __builtin_amdgcn_s_setprio(0);
  if (w < 7) {
#pragma unroll
    for (int gi = 4; gi < 7; ++gi) P1_LOAD4(gi);
    int sg = w - rot7; if (sg < 0) sg += 7;
    LAS unsigned char* h2w = L + L_HT + 14336 + sg * 2048 + lane * 16;
    LAS unsigned char* h2r = L + L_HT + 14336 + lane * 16;
    LAS unsigned char* mc2 = L + L_MOD + w * 256 + g * 32;
    v4u xa[2][2];
#define X1_LOAD(P) do { _Pragma("unroll") for (int t = 0; t < 2; ++t) { const int so_ = WS_X1 + (p * 4 + 2 * (P) + t) * 14336 + w * 2048; \
      xa[t][0] = LD128(lane * 16, so_); xa[t][1] = LD128(lane * 16 + 1024, so_); } } while (0)
#define X1_CHECK(P) PIN(xa[0][0]); PIN(xa[0][1]); PIN(xa[1][0]); PIN(xa[1][1]); RETRY(max4(max4(max4(max4(0u, xa[0][0]), xa[0][1]), xa[1][0]), xa[1][1]) == POISON, X1_LOAD(P))
#define LN2(P) do { \
    const f32x4 st0 = *(LAS f32x4*)(L + L_ST + ((P) * 32 + r) * 16), st1 = *(LAS f32x4*)(L + L_ST + ((P) * 32 + 16 + r) * 16); \
    _Pragma("unroll") for (int i = 0; i < 2; ++i) { \
      const f32x4 s0_ = *(LAS f32x4*)(mc2 + i * 128), s1_ = *(LAS f32x4*)(mc2 + i * 128 + 16); \
      const f32x4 c0_ = *(LAS f32x4*)(mc2 + 1792 + i * 128), c1_ = *(LAS f32x4*)(mc2 + 1792 + i * 128 + 16); \
      _Pragma("unroll") for (int t = 0; t < 2; ++t) { \
        const f32x2 a_ = t ? f32x2{st1.x, st1.y} : f32x2{st0.x, st0.y}, b_ = t ? f32x2{st1.z, st1.w} : f32x2{st0.z, st0.w}; \
        const f32x2 v0 = fma2(fma2(unpk(xa[t][i][0]), a_, b_), f32x2{c0_.x, c0_.y}, f32x2{s0_.x, s0_.y}); \
        const f32x2 v1 = fma2(fma2(unpk(xa[t][i][1]), a_, b_), f32x2{c0_.z, c0_.w}, f32x2{s0_.z, s0_.w}); \
        const f32x2 v2 = fma2(fma2(unpk(xa[t][i][2]), a_, b_), f32x2{c1_.x, c1_.y}, f32x2{s1_.x, s1_.y}); \
        const f32x2 v3 = fma2(fma2(unpk(xa[t][i][3]), a_, b_), f32x2{c1_.z, c1_.w}, f32x2{s1_.z, s1_.w}); \
        *(LAS v4u*)(h2w + (2 * (P) + t) * 14336 + i * 1024) = v4u{pack2(v0.x, v0.y), pack2(v1.x, v1.y), pack2(v2.x, v2.y), pack2(v3.x, v3.y)}; } } \
    SCHED(); } while (0)
#define U_MFMA(P, EXTRA) do { \
      v4u bf[2][2]; \
      _Pragma("unroll") for (int t = 0; t < 2; ++t) { a0[t] = f32x4{0, 0, 0, 0}; a1[t] = f32x4{0, 0, 0, 0}; \
        bf[0][t] = *(LAS v4u*)(h2r + (2 * (P) + t) * 14336); bf[1][t] = *(LAS v4u*)(h2r + (2 * (P) + t) * 14336 + 1024); } \
      SCHED(); \
      _Pragma("unroll") for (int s = 0; s < 14; ++s) { \
        _Pragma("unroll") for (int t = 0; t < 2; ++t) { \
          a0[t] = mfma(fa[s], bf[s & 1][t], a0[t]); \
          a1[t] = mfma(fb[s], bf[s & 1][t], a1[t]); \
          if (s < 12) bf[s & 1][t] = *(LAS v4u*)(h2r + (2 * (P) + t) * 14336 + (s + 2) * 1024); } \
        EXTRA; \
        SCHED(); } } while (0)
#define U_SILU(P) do { _Pragma("unroll") for (int t = 0; t < 2; ++t) { v4u hv; \
      _Pragma("unroll") for (int e = 0; e < 4; ++e) { \
        const f32x2 u_ = {a0[t][e], a1[t][e]}; \
        const f32x2 t_ = u_ * f32x2{-1.4426950408889634f, -1.4426950408889634f}; \
        const f32x2 d_ = f32x2{__builtin_amdgcn_exp2f(t_.x), __builtin_amdgcn_exp2f(t_.y)} + f32x2{1.f, 1.f}; \
        const f32x2 o_ = u_ * f32x2{__builtin_amdgcn_rcpf(d_.x), __builtin_amdgcn_rcpf(d_.y)}; \
        hv[e] = pack2(o_.x, o_.y); \
        if ((P) == 1) hv[e] = sane(hv[e]); } \
      ST128(hv, lane * 16, WS_H + (((p * 4 + 2 * (P) + t) * 8 + j) * 7 + w) * 1024); } SCHED(); } while (0)
#define WE_ALL() do { _Pragma("unroll") for (int q = 0; q < W2E; ++q) WE_LOAD(q); SCHED(); } while (0)
    f32x4 a0[2], a1[2];
    pace(t_pub, DLY_U);
    __builtin_amdgcn_s_setprio(3);
    X1_LOAD(0);
    WAITVM0();
    X1_CHECK(0);
    BAR();
    STAMP(11);
    LN2(0);
    ST128(pz, lane * 16, WS_H + (((p * 4 + 2) * 8 + j) * 7 + w) * 1024);
    ST128(pz, lane * 16, WS_H + (((p * 4 + 3) * 8 + j) * 7 + w) * 1024);
    X1_LOAD(1);
    BAR();
    STAMP(12);
    if (w < 4) {
      __builtin_amdgcn_s_setprio(1);
      U_MFMA(0, (void)0); X1_CHECK(1); U_SILU(0); LN2(1); WE_ALL();
    } else {
      __builtin_amdgcn_s_setprio(0);
      X1_CHECK(1); LN2(1); U_MFMA(0, (void)0); U_SILU(0);
    }
    STAMP(16);
    BAR();
    STAMP(13);
#define HFLAG() do { VM0(); if (lane == 0) { if (ARRIVE(4) == 6u) ST32(tag, WS_HF + (p * 8 + j) * 128, 0); } } while (0)
    if (w >= 4) { HFLAG(); WE_ALL(); }
    U_MFMA(1, if (s == 5 && w < 4) HFLAG());
    U_SILU(1);
    tH3 = now32();
    hf1 = HF_LD(0);
#pragma unroll
    for (int q = 0; q < 7 - W2E; ++q) WL_LOAD(q);
    STAMP(17);
  } else {
    v4u st[8];
#define ST_LOAD() do { _Pragma("unroll") for (int q = 0; q < 8; ++q) st[q] = LD128(g * 2048 + r * 16 + q * 256, WS_ST + p * 8192); } while (0)
    pace(t_pub, DLY_U);
    __builtin_amdgcn_s_setprio(3);
    ST_LOAD();
    RETRY(max4(MAX7(st), st[7]) == POISON, ST_LOAD());
    f32x4 a = __builtin_bit_cast(f32x4, st[0]);
#pragma unroll
    for (int q = 1; q < 8; ++q) a += __builtin_bit_cast(f32x4, st[q]);
    const float mu = (a.x + a.z) * (1.f / D);
    const float rstd = __builtin_amdgcn_rsqf(fmaxf((a.y + a.w) * (1.f / D) - mu * mu, 0.f) + 1e-5f);
    *(LAS f32x4*)(L + L_ST + lane * 16) = f32x4{rstd, rstd, -mu * rstd, -mu * rstd};
    __builtin_amdgcn_s_setprio(0);
    BAR();
    STAMP(11);
    BAR();
#pragma unroll
    for (int s = 0; s < W2E; ++s) WE_LOAD(s);
#pragma unroll
    for (int q = 0; q < 7 - W2E; ++q) WL_LOAD(q);
    BAR();
    CNT_WAIT(4, 7u);
    tH3 = now32();
    pace(tH3, 800);
    hf1 = HF_LD(0);
    tH3 += 1000u;
  }
  HF_WAIT(hf1, 0);
  H_LOAD(h0, 0);
  H_LOAD(h1, 1);

#define W2F(s, a) ((s) < W2E ? we[(s) < W2E ? (s) : 0][a] : (((a) & 1) ? fb[2 * ((s) >= W2E ? (s) - W2E : 0) + ((a) >> 1)] : fa[2 * ((s) >= W2E ? (s) - W2E : 0) + ((a) >> 1)]))
#define RED_WRITE(red, acc) do { _Pragma("unroll") for (int a = 0; a < 4; ++a) *(LAS f32x4*)((red) + (w * 4 + a) * 1024 + lane * 16) = acc[a]; } while (0)
#define OUT_STORE(y, T, a) do { if ((a) < 3 || g < 2) __builtin_amdgcn_raw_buffer_store_b64(y, rOUT, r * 896 + g * 8, (64 * p + 16 * (T)) * 896 + 112 * j + (a) * 32, 0); } while (0)
#define RED_FINISH(yo, red, T, a) do { \
    f32x4 s_ = *(LAS f32x4*)((red) + (a) * 1024 + lane * 16); \
    _Pragma("unroll") for (int ww = 1; ww < 8; ++ww) s_ += *(LAS f32x4*)((red) + (ww * 4 + (a)) * 1024 + lane * 16); \
    const v2u x1 = *(LAS v2u*)(L + L_X1 + (T) * 1792 + min(2 * (a) + (g >> 1), 6) * 256 + r * 16 + (g & 1) * 8); \
    const f32x4 gb = *(LAS f32x4*)(L + L_G + 256 + (a) * 64 + g * 16); \
    s_ = __builtin_elementwise_fma(gb, s_, f32x4{bflo(x1.x), bfhi(x1.x), bflo(x1.y), bfhi(x1.y)}); \
    yo = v2u{pack2(s_[0], s_[1]), pack2(s_[2], s_[3])}; \
  } while (0)
#define D_TILE(acc, h) do { \
    _Pragma("unroll") for (int a = 0; a < 4; ++a) acc[a] = f32x4{0, 0, 0, 0}; \
    _Pragma("unroll") for (int s = 0; s < 7; ++s) { \
      _Pragma("unroll") for (int a = 0; a < 4; ++a) acc[a] = mfma(W2F(s, a), h[s], acc[a]); \
      SCHED(); } } while (0)
  {
    SCHED();
    STAMP(20);
    f32x4 ac[4];
    D_TILE(ac, h0);
    pace(tH3, DLY_D);
    H_LOAD(h0, 2);
    RED_WRITE(L + L_R01, ac);
    SCHED();
    D_TILE(ac, h1);
    H_LOAD(h1, 3);
    RED_WRITE(L + L_R01 + 32768, ac);
    SCHED();
    BAR();
    STAMP(21);
    v2u y01, y23;
    RED_FINISH(y01, L + L_R01 + (w >> 2) * 32768, (w >> 2), (w & 3));
    PIN(y01);
    STAMP(24);
    RETRY(MAX7(h0) == POISON, H_LOAD(h0, 2));
    D_TILE(ac, h0);
    RED_WRITE(L + L_R23, ac);
    RETRY(MAX7(h1) == POISON, H_LOAD(h1, 3));
    OUT_STORE(y01, (w >> 2), (w & 3));
    D_TILE(ac, h1);
    RED_WRITE(L + L_R23 + 32768, ac);
    BAR();
    STAMP(26);
    RED_FINISH(y23, L + L_R23 + (w >> 2) * 32768, 2 + (w >> 2), (w & 3));
    OUT_STORE(y23, 2 + (w >> 2), (w & 3));
    STAMP(27);
  }
  asm volatile("adaln_lbl1:");
  if (b == 0 && tid == 0) ST32(tag, WS_GEN, 0);
}

extern "C" void kernel_launch(void* const* d_in, const int* in_sizes, int n_in, void* d_out, int out_size, void* d_ws,
                              size_t ws_size, hipStream_t stream) {
  adaln_block<<<256, 512, 0, stream>>>((const u16*)d_in[0], (const u16*)d_in[1], (const u16*)d_in[2], (const u16*)d_in[4],
                                       (const u16*)d_in[5], (const u16*)d_in[6], (const u16*)d_in[7], (u16*)d_out,
                                       (unsigned char*)d_ws);
}
```

```cpp
#include <hip/hip_runtime.h>
#include <stdint.h>

typedef unsigned v4u __attribute__((ext_vector_type(4)));
typedef unsigned v2u __attribute__((ext_vector_type(2)));
typedef float f32x4 __attribute__((ext_vector_type(4)));
typedef float f32x2 __attribute__((ext_vector_type(2)));
typedef short bf16x8 __attribute__((ext_vector_type(8)));
typedef __bf16 bf16x2 __attribute__((ext_vector_type(2)));
typedef unsigned short u16;
typedef unsigned long long u64;
#define LAS __attribute__((address_space(3)))
#define GAS __attribute__((address_space(1)))
typedef __amdgpu_buffer_rsrc_t rsrc_t;

constexpr int DLY_Q = 7800, DLY_M = 7800, DLY_O = 900, DLY_U = 1300, DLY_H = 500;
constexpr int PRECHK = 3;
constexpr int MODM = 7, UNP = 8;
constexpr int QP1 = 2, QP2 = 2, QP3 = 5, QP4 = 6;
constexpr int W2E = 4, WE0 = 0;
constexpr int UD = 2;
constexpr int CODEPF = 4;

constexpr int S = 2048, D = 448, F = 1792;
constexpr int SC1 = 16;
constexpr unsigned POISON = 0xFFFFFFFFu;
constexpr unsigned SPIN_CAP = 1u << 18;

constexpr int WS_GEN = 0;
constexpr int WS_READY = 11 << 20;
constexpr int WS_RP = WS_READY + 16384;
constexpr int WS_MODG = 8192;
constexpr int WS_OBUF = 65536;
constexpr int WS_X1 = WS_OBUF + 128 * 14336;
constexpr int WS_ST = WS_X1 + 128 * 14336;
constexpr int WS_H = WS_ST + 128 * 2048;
constexpr int WS_PO = 12 << 20;
constexpr int WS_P1 = WS_PO + 401408;
constexpr int WS_P2 = WS_P1 + 1605632;
constexpr int WS_PQ = WS_P2 + 1605632;
constexpr int WS_END = WS_PO + (8 << 20);
static_assert(WS_H + 128 * 57344 <= WS_READY && WS_PQ + 48 * 14 * 2048 <= WS_PO + (8 << 20), "ws");

constexpr int L_HT = 0;
constexpr int L_WO = 71680;
constexpr int L_KV = L_WO + 50176;
constexpr int L_QF = L_KV + 20480;
constexpr int L_X1 = L_QF + 4096;
constexpr int L_MOD = L_X1 + 7168;
constexpr int L_G = L_MOD + 3584;
constexpr int L_ST = L_G + 512;
constexpr int L_CNT = L_ST + 2048;
constexpr int L_END = L_CNT + 32;
constexpr int L_MRED = L_KV;
constexpr int L_OX = L_HT;
constexpr int L_R01 = L_WO;
constexpr int L_R23 = L_HT;
static_assert(L_END + 1024 <= 163840 && L_R01 + 65536 <= L_X1, "lds");

__device__ __forceinline__ float bflo(unsigned u) { return __builtin_bit_cast(float, u << 16); }
__device__ __forceinline__ float bfhi(unsigned u) { return __builtin_bit_cast(float, u & 0xFFFF0000u); }
__device__ __forceinline__ float asf(unsigned u) { return __builtin_bit_cast(float, u); }
__device__ __forceinline__ unsigned asu(float f) { return __builtin_bit_cast(unsigned, f); }
__device__ __forceinline__ unsigned pack2(float a, float b) {
  bf16x2 r = __builtin_convertvector(f32x2{a, b}, bf16x2);
  return __builtin_bit_cast(unsigned, r);
}
__device__ __forceinline__ f32x2 unpk(unsigned u) { return f32x2{bflo(u), bfhi(u)}; }
__device__ __forceinline__ f32x2 fma2(f32x2 a, f32x2 b, f32x2 c) { return __builtin_elementwise_fma(a, b, c); }
__device__ __forceinline__ float dot2(unsigned a, unsigned b, float c) {
  return __builtin_amdgcn_fdot2_f32_bf16(__builtin_bit_cast(bf16x2, a), __builtin_bit_cast(bf16x2, b), c, false);
}
__device__ __forceinline__ unsigned sane(unsigned u) { return u < 0xFFFFFFFEu ? u : 0xFFFFFFFEu; }
__device__ __forceinline__ f32x4 mfma(v4u a, v4u b, f32x4 c) {
  return __builtin_amdgcn_mfma_f32_16x16x32_bf16(__builtin_bit_cast(bf16x8, a), __builtin_bit_cast(bf16x8, b), c, 0, 0, 0);
}
__device__ __forceinline__ rsrc_t mk(const void* p, int bytes) {
  return __builtin_amdgcn_make_buffer_rsrc((void*)p, 0, bytes, 0x00020000);
}
#define SPIN _Pragma("nounroll") for (unsigned spin = 0; spin < SPIN_CAP; ++spin)
#define SCHED() __builtin_amdgcn_sched_barrier(0)
#define CFENCE() asm volatile("" ::: "memory")
#define LGKM0() asm volatile("s_waitcnt lgkmcnt(0)" ::: "memory")
#define VM0() asm volatile("s_waitcnt vmcnt(0)" ::: "memory")
#define BAR() do { LGKM0(); __builtin_amdgcn_s_barrier(); CFENCE(); } while (0)
#define PIN(x) asm volatile("" : "+v"(x))
#define USE(x) asm volatile("" :: "v"(x) : "memory")
#define LD128(voff, soff) __builtin_amdgcn_raw_buffer_load_b128(rWS, (voff), (soff), SC1)
#define ST128(v, voff, soff) do { v4u d_ = (v); asm volatile("" : "+v"(d_)); __builtin_amdgcn_raw_buffer_store_b128(d_, rWS, (voff), (soff), SC1); asm volatile("s_nop 1" :: "v"(d_)); } while (0)
#define ST64(v, voff, soff) __builtin_amdgcn_raw_buffer_store_b64((v), rWS, (voff), (soff), SC1)
#define ST32(v, voff, soff) __builtin_amdgcn_raw_buffer_store_b32((v), rWS, (voff), (soff), SC1)
#define WAITVM0() __builtin_amdgcn_s_waitcnt(0x0F70)
#define RETRY2(first, badexpr, reload) do { if (__builtin_expect(__any(first), 0)) { SPIN { CFENCE(); __builtin_amdgcn_s_sleep(2); reload; WAITVM0(); if (!__any(badexpr)) break; } } } while (0)
#define RETRY(badexpr, reload) do { if (__builtin_expect(__any(badexpr), 0)) { SPIN { CFENCE(); __builtin_amdgcn_s_sleep(2); reload; WAITVM0(); if (!__any(badexpr)) break; } } } while (0)

template <int CTRL>
__device__ __forceinline__ float dpp_add(float v) {
  int t = __builtin_amdgcn_update_dpp(0, __builtin_bit_cast(int, v), CTRL, 0xF, 0xF, false);
  return v + __builtin_bit_cast(float, t);
}
__device__ __forceinline__ float fold16(float a, float b) {
  auto t = __builtin_amdgcn_permlane16_swap(asu(a), asu(b), false, false);
  const unsigned t0 = t[0], t1 = t[1];
  return asf(t0) + asf(t1);
}
__device__ __forceinline__ float fold32(float a, float b) {
  auto t = __builtin_amdgcn_permlane32_swap(asu(a), asu(b), false, false);
  const unsigned t0 = t[0], t1 = t[1];
  return asf(t0) + asf(t1);
}
__device__ __forceinline__ float rows_max(float v) {
  auto t = __builtin_amdgcn_permlane16_swap(asu(v), asu(v), false, false);
  unsigned t0 = t[0], t1 = t[1];
  v = fmaxf(asf(t0), asf(t1));
  auto q = __builtin_amdgcn_permlane32_swap(asu(v), asu(v), false, false);
  t0 = q[0]; t1 = q[1];
  return fmaxf(asf(t0), asf(t1));
}
__device__ __forceinline__ float rows_sum(float v) { v = fold16(v, v); return fold32(v, v); }
__device__ __forceinline__ unsigned max4(unsigned m, v4u v) { return max(max(max(m, v.x), v.y), max(v.z, v.w)); }
__device__ __forceinline__ unsigned now32() { return (unsigned)__builtin_amdgcn_s_memtime(); }
__device__ __forceinline__ void pace(unsigned t0, int dly) {
  _Pragma("nounroll") for (unsigned spin = 0; spin < 4096; ++spin) {
    if ((int)(now32() - t0) > dly) break;
    __builtin_amdgcn_s_sleep(1);
  }
}
__device__ __forceinline__ void rows8(unsigned (&r)[8], const unsigned char* src, int soff, unsigned (&vo)[8]) {
  const GAS unsigned char* pb = (const GAS unsigned char*)src + soff;
  asm volatile("" : "+s"(pb), "+v"(vo[0]), "+v"(vo[1]), "+v"(vo[2]), "+v"(vo[3]), "+v"(vo[4]), "+v"(vo[5]), "+v"(vo[6]), "+v"(vo[7]));
#pragma unroll
  for (int jj = 0; jj < 8; ++jj) r[jj] = *(const GAS unsigned*)(pb + vo[jj]);
}
__device__ __forceinline__ void xpose(const unsigned (&r)[8], v4u& f0, v4u& f1) {
#pragma unroll
  for (int m = 0; m < 4; ++m) {
    f0[m] = __builtin_amdgcn_perm(r[2 * m + 1], r[2 * m], 0x05040100u);
    f1[m] = __builtin_amdgcn_perm(r[2 * m + 1], r[2 * m], 0x07060302u);
  }
}

typedef short s16x4 __attribute__((ext_vector_type(4)));
__device__ __forceinline__ v2u trread(LAS unsigned char* p) {
  return __builtin_bit_cast(v2u, __builtin_amdgcn_ds_read_tr16_b64_v4i16((LAS s16x4*)p));
}


__global__ void __launch_bounds__(512, 2)
adaln_block(const u16* __restrict__ x, const u16* __restrict__ wqkv, const u16* __restrict__ wo, const u16* __restrict__ w1,
            const u16* __restrict__ w2, const u16* __restrict__ cvec, const u16* __restrict__ wmod, u16* out,
            unsigned char* ws) {
  __shared__ __attribute__((aligned(16))) unsigned char smem[L_END];
  LAS unsigned char* L = (LAS unsigned char*)smem;

  const int b = blockIdx.x;
  const int tid = threadIdx.x, lane = tid & 63, w = __builtin_amdgcn_readfirstlane(tid >> 6);
  const unsigned t_start = now32();
  unsigned cpf = 0;
  {
    const GAS unsigned char* pc; unsigned len;
    asm volatile("s_getpc_b64 %0\nadaln_lbl0:\n s_mov_b32 %1, adaln_lbl1-adaln_lbl0" : "=s"(pc), "=s"(len));
    const unsigned off = (unsigned)(tid ^ 256) * 128u;
    cpf = *(const GAS unsigned*)(pc + ((off < len && (b >> 3) % CODEPF == 0) ? off : 0u));
  }
#define CPF_USE() asm volatile("" :: "v"(cpf))
  const int xcd = b & 7, li = b >> 3, grp = li >> 3, j = li & 7, p = xcd * 4 + grp;
  const int r = lane & 15, g = lane >> 4;
  const int lc = lane < 56 ? lane : 55;
  const int ci = b / 3;
  const bool mtask = b < 252 && b - 3 * ci == 1;

  const rsrc_t rX = mk(x, S * D * 2), rWM = mk(wmod, D * 6 * D * 2), rC = mk(cvec, D * 2), rWS = mk(ws, WS_END),
               rOUT = mk(out, S * D * 2);
  const v4u pz = {POISON, POISON, POISON, POISON};
  LAS unsigned* cnt = (LAS unsigned*)(L + L_CNT);
  cnt[tid & 7] = 0u;
  unsigned tag = *(const __attribute__((address_space(4))) unsigned*)(ws + WS_GEN);
#define BAR0() do { asm volatile("" : "+s"(tag)); tag += 1u; BAR(); } while (0)
  const int head = w & 1, T_ = w >> 1;
  v4u fa[14], fb[14];
  unsigned ck[14];
  const int fr14 = g * 224 + min(r, 13) * 16;
  const int pq_base = WS_PQ + (j * 6 + w) * 28672;
#define PQ_LOAD(s) do { const int so_ = pq_base + (s) * 2048; \
    fa[s] = LD128(fr14, so_); fb[s] = LD128(fr14 + 896, so_); ck[s] = __builtin_amdgcn_raw_buffer_load_b32(rWS, (fr14 >> 2) + 1792, so_, SC1); } while (0)
#define CSUM(a, b) (((a).x + (a).y + (a).z) + ((a).w + (b).x + (b).y) + ((b).z + (b).w + tag))
#define PQ_CHECK(s) RETRY(CSUM(fa[s], fb[s]) != ck[s], PQ_LOAD(s))
#define ARRIVE(i) __hip_atomic_fetch_add(cnt + (i), 1u, __ATOMIC_RELAXED, __HIP_MEMORY_SCOPE_WORKGROUP)
#define CNT_WAIT(i, n) do { _Pragma("nounroll") for (unsigned spin = 0; spin < (SPIN_CAP << 8); ++spin) { \
    if (__builtin_amdgcn_readfirstlane(*(volatile LAS unsigned*)(cnt + (i))) == (n)) break; } CFENCE(); } while (0)
#define READY_ARRIVE() do { if (lane == 0) { if (ARRIVE(1) == 3u) ST32(tag, WS_READY + (b >> 2) * 128 + (b & 3) * 4, 0); } } while (0)
#define MOD_LOAD(s0) do { _Pragma("unroll") for (int q = 0; q < 4; ++q) \
    m[q] = LD128(lc * 32 + (q & 1) * 16, WS_MODG + ((q >> 1) * 2688 + ((s0) + ((w >> 1) & 1)) * 448 + (w & 1) * 224) * 8); } while (0)
#define MX(q) ((m[q].y ^ tag) | (m[q].w ^ tag))
#define MOD_BAD ((MX(0) | MX(1) | MX(2) | MX(3)) != 0u)
#define MOD_PUT() do { \
    unsigned k0 = pack2(asf(m[0].x) + asf(m[2].x), asf(m[0].z) + asf(m[2].z)), k1 = pack2(asf(m[1].x) + asf(m[3].x), asf(m[1].z) + asf(m[3].z)); \
    if (w & 2) { k0 = pack2(1.f + bflo(k0), 1.f + bfhi(k0)); k1 = pack2(1.f + bflo(k1), 1.f + bfhi(k1)); } \
    *(LAS f32x4*)(L + L_MOD + (w - 4) * 896 + lc * 16) = f32x4{bflo(k0), bfhi(k0), bflo(k1), bfhi(k1)}; } while (0)
  const int p1_base = WS_P1 + (j * 7 + w) * 28672;
#define P1_LOAD4(gi) do { const int so_ = p1_base + (gi) * 4096; \
    fa[2 * (gi)] = LD128(lane * 16, so_); fb[2 * (gi)] = LD128(lane * 16 + 1024, so_); \
    fa[2 * (gi) + 1] = LD128(lane * 16 + 2048, so_); fb[2 * (gi) + 1] = LD128(lane * 16 + 3072, so_); } while (0)
  LAS unsigned char* hre = L + L_HT + (g * 16 + (r ^ g)) * 16;
  LAS unsigned char* hro = L + L_HT + (g * 16 + (r ^ (4 | g))) * 16;
  v4u qmine, wt[13];
  const int wo0 = min((w & 3) * 13, 36);
  f32x4 bias0, bias1;
#define MK_BIAS() do { const bool prev = (p + T_) > 0; \
    _Pragma("unroll") for (int e = 0; e < 4; ++e) { const int d1 = r - (4 * g + e); const bool ev = !(d1 & 1); \
      bias0[e] = (prev && d1 <= -2 && ev) ? 0.f : -INFINITY; bias1[e] = (d1 >= 0 && ev) ? 0.f : -INFINITY; } \
    PIN(bias0); PIN(bias1); } while (0)
  asm volatile("" :: "s"(x), "s"(wqkv), "s"(wo), "s"(w1), "s"(w2), "s"(cvec), "s"(wmod), "s"(out), "s"(ws));
  if (w < 4) {
    __builtin_amdgcn_s_setprio(3);
    unsigned v2[8];
    const int lu = min(w * 256 + b, 881);
    const int m32 = lane & 31, gh = lane >> 5;
    const unsigned char* src; int rowb, lcol, ks;
    unsigned la[8], lb[8];
#define LATE_LOAD() do { \
    if (lu < 392) { src = (const unsigned char*)w1; rowb = 3584; lcol = lu / 14; ks = lu % 14; } \
    else if (lu < 784) { src = (const unsigned char*)w2; rowb = 896; lcol = (lu - 392) / 56; ks = (lu - 392) % 56; } \
    else { src = (const unsigned char*)wo; rowb = 896; lcol = (lu - 784) / 14; ks = (lu - 784) % 14; } \
    _Pragma("unroll") for (int q = 0; q < 8; ++q) v2[q] = (gh * 8 + q) * rowb + m32 * 4; \
    rows8(la, src, ks * 32 * rowb + lcol * 128, v2); \
    rows8(lb, src, (ks * 32 + 16) * rowb + lcol * 128, v2); SCHED(); } while (0)
    if (mtask) {
      v4u wm[7];
      const int row0 = (ci & 1) * 224 + 56 * w;
      const unsigned cr = __builtin_amdgcn_raw_buffer_load_b16(rC, lc * 2, row0 * 2, 0);
#pragma unroll
      for (int i = 0; i < 7; ++i)
        wm[i] = __builtin_amdgcn_raw_buffer_load_b128(rWM, (lane >> 3) * 5376 + (lane & 7) * 16, (row0 + 8 * i) * 5376 + (ci >> 1) * 128, 0);
      SCHED();
      LATE_LOAD();
      BAR0();
      const float cv = bflo(cr);
      const float sv0 = cv * __builtin_amdgcn_rcpf(1.f + __expf(-cv));
      const int svi = (int)(pack2(sv0, sv0) << 16);
      f32x2 acc[4];
#pragma unroll
      for (int i = 0; i < 7; ++i) {
        const float sv = asf(__builtin_amdgcn_ds_bpermute((lane >> 3) * 4 + i * 32, svi));
#pragma unroll
        for (int e = 0; e < 4; ++e) acc[e] = i ? fma2(f32x2{sv, sv}, unpk(wm[i][e]), acc[e]) : f32x2{sv, sv} * unpk(wm[i][e]);
      }
      f32x2 h0 = {fold32(acc[0].x, acc[2].x), fold32(acc[0].y, acc[2].y)}, h1 = {fold32(acc[1].x, acc[3].x), fold32(acc[1].y, acc[3].y)};
      f32x2 q0 = {dpp_add<0x128>(fold16(h0.x, h1.x)), dpp_add<0x128>(fold16(h0.y, h1.y))};
      *(LAS f32x2*)(L + L_MRED + w * 256 + (r & 7) * 32 + g * 8) = q0;
      LGKM0();
      unsigned old = 0;
      if (lane == 0) old = ARRIVE(0);
      if (__builtin_amdgcn_readfirstlane(old) == 3u) {
        CFENCE();
        float s0 = 0.f;
#pragma unroll
        for (int ww = 0; ww < 4; ++ww) s0 += *(LAS float*)(L + L_MRED + ww * 256 + lane * 4);
        ST64((v2u{asu(s0), tag}), lane * 8, WS_MODG + ((ci & 1) * 2688 + (ci >> 1) * 64) * 8);
      }
    } else {
      const int qu = min((ci * 2 + ((b - 3 * ci) >> 1)) * 4 + w, 671), qk = qu / 48, hs = qu - qk * 48;
      unsigned uq[8];
#pragma unroll
      for (int q = 0; q < 8; ++q) v2[q] = (8 * g + q) * 2688 + min(r, 13) * 4;
      rows8(uq, (const unsigned char*)wqkv, qk * (32 * 2688) + hs * 56, v2);
      SCHED();
      LATE_LOAD();
      BAR0();
      v4u f0, f1;
      xpose(uq, f0, f1);
      const int so = WS_PQ + ((((hs & 15) >> 1) * 6 + ((hs >> 4) == 1 ? 0 : ((hs >> 4) == 2 ? 2 : 4)) + (hs & 1)) * 14 + qk) * 2048;
      if (b < 252) { ST128(f0, fr14, so); ST128(f1, fr14 + 896, so); ST32(CSUM(f0, f1), (fr14 >> 2) + 1792, so); }
    }
    {
      const int col = 64 * lcol + 2 * m32;
      const int jj = col / 56, cc = col - jj * 56;
      int dst, fs, gs;
      if (lu < 392) { fs = 1024; gs = 256; dst = WS_P1 + ((col >> 5) * 14 + ks) * 2048 + ((col & 31) >> 1) * 16; }
      else { const int hc = cc >> 5; fs = hc ? 768 : 1024; gs = hc ? 192 : 256;
             dst = (lu < 784 ? WS_P2 + (jj * 56 + ks) * 3584 : WS_PO + (jj * 14 + ks) * 3584) + hc * 2048 + ((cc & 31) >> 1) * 16; }
      dst += gh * gs;
      if (w * 256 + b < 882) {
        v4u f0, f1, f2, f3;
        xpose(la, f0, f1); xpose(lb, f2, f3);
        PIN(f0); PIN(f1); PIN(f2); PIN(f3);
        ST128(f0, dst, 0); ST128(f1, dst + fs, 0); ST128(f2, dst + 2 * gs, 0); ST128(f3, dst + 2 * gs + fs, 0);
      }
    }
    __builtin_amdgcn_s_setprio(0);
    pace(t_start, DLY_Q);
    v4u qa[7], qb[7];
    unsigned qc[7];
#define QQ_LOAD(s) do { const int so_ = WS_PQ + (j * 6 + 4 + head) * 28672 + ((w >> 1) * 7 + (s)) * 2048; \
    qa[s] = LD128(fr14, so_); qb[s] = LD128(fr14 + 896, so_); qc[s] = __builtin_amdgcn_raw_buffer_load_b32(rWS, (fr14 >> 2) + 1792, so_, SC1); } while (0)
#pragma unroll
    for (int s = 0; s < PRECHK; ++s) PQ_LOAD(s);
    SCHED(); USE(fa[0]); READY_ARRIVE();
#pragma unroll
    for (int s = 0; s < 7; ++s) QQ_LOAD(s);
#pragma unroll
    for (int s = PRECHK; s < 14; ++s) { PQ_LOAD(s); SCHED(); }
    MK_BIAS();
#pragma unroll
    for (int s = 0; s < PRECHK; ++s) PQ_CHECK(s);
#pragma unroll
    for (int u = 0; u < 7; ++u) {
      RETRY(CSUM(qa[u], qb[u]) != qc[u], QQ_LOAD(u));
      LAS unsigned char* d_ = L + L_WO + head * 25088 + (w >> 1) * 12544 + fr14;
      *(LAS v4u*)(d_ + u * 1792) = qa[u]; *(LAS v4u*)(d_ + u * 1792 + 896) = qb[u];
    }
    CPF_USE();
    BAR();
    __builtin_amdgcn_s_setprio(2);
    f32x4 a0[5], a1[5];
    v4u bx[2][5];
    unsigned bad = 0u, cs = 0u;
#define KV_GEMM(FAST) do { \
    _Pragma("unroll") for (int t = 0; t < 5; ++t) { a0[t] = f32x4{0, 0, 0, 0}; a1[t] = f32x4{0, 0, 0, 0}; bx[0][t] = *(LAS v4u*)(hre + t * 14336); } \
    _Pragma("unroll") for (int t = 0; t < 5; ++t) bx[1][t] = *(LAS v4u*)(hro + t * 14336 + 1024); \
    SCHED(); \
    _Pragma("unroll") for (int s = 0; s < 14; ++s) { \
      _Pragma("unroll") for (int t = 0; t < 5; ++t) { \
        a0[t] = mfma(fa[s], bx[s & 1][t], a0[t]); \
        a1[t] = mfma(fb[s], bx[s & 1][t], a1[t]); \
        if (s < 12) bx[s & 1][t] = *(LAS v4u*)(((s & 1) ? hro : hre) + t * 14336 + (s + 2) * 1024); \
        if (FAST && s >= PRECHK) { \
          if (t == 0) cs = fa[s].x + fa[s].y + tag; \
          if (t == 1) cs = cs + fa[s].z + fa[s].w; \
          if (t == 2) cs = cs + fb[s].x + fb[s].y; \
          if (t == 3) cs = cs + fb[s].z + fb[s].w; \
          if (t == 4) { bad |= cs ^ ck[s]; PIN(bad); } else PIN(cs); } \
        SCHED(); } } } while (0)
    KV_GEMM(true);
    if (__builtin_expect(__any(bad != 0u), 0)) {
#pragma unroll
      for (int s = PRECHK; s < 14; ++s) PQ_LOAD(s);
      WAITVM0();
#pragma unroll
      for (int s = PRECHK; s < 14; ++s) PQ_CHECK(s);
      KV_GEMM(false);
    }
    CNT_WAIT(7, 1u);
#pragma unroll
    for (int gi = 0; gi < QP1; ++gi) P1_LOAD4(gi);
    SCHED();
    LAS unsigned char* dst = L + L_KV + head * 2048 + (w < 2 ? lane * 16 : 1024 + r * 64 + g * 16);
#pragma unroll
    for (int t = 0; t < 5; ++t)
      *(LAS v4u*)(dst + t * 4096) = v4u{pack2(a0[t][0], a1[t][0]), pack2(a0[t][1], a1[t][1]), pack2(a0[t][2], a1[t][2]), pack2(a0[t][3], a1[t][3])};
    qmine = pz;
  } else {
    const int q = w - 4;
    __builtin_amdgcn_s_sleep(1);
    __builtin_amdgcn_s_setprio(2);
    v4u xr[20];
    const int xvo = lane < 56 ? lane * 16 : 0x40000000;
    const int cq = (j & 3) << 2;
#define X_LOAD(i) xr[i] = __builtin_amdgcn_raw_buffer_load_b128(rX, xvo + ((i) & 3) * 896, \
      (i) < 16 ? (64 * p + 16 * q + (((i) & 12) ^ cq)) * 896 : max(64 * p - 16 + 4 * q, 0) * 896, 0)
#pragma unroll
    for (int i = 0; i < 8; ++i) X_LOAD(i);
    BAR0();
    float u[10];
    v4u m[4];
    f32x2 xn[20][4];
#pragma unroll
    for (int mm = 0; mm < 10; ++mm) {
      if (mm < 6) { X_LOAD(8 + 2 * mm); X_LOAD(9 + 2 * mm); }
      if (mm == MODM) { pace(t_start, DLY_M); MOD_LOAD(0); }
      SCHED();
      float t[2];
#pragma unroll
      for (int h = 0; h < 2; ++h) {
        float s1 = 0.f, s2 = 0.f;
#pragma unroll
        for (int e = 0; e < 4; ++e) {
          s1 = dot2(xr[2 * mm + h][e], 0x3F803F80u, s1); s2 = dot2(xr[2 * mm + h][e], xr[2 * mm + h][e], s2);
          if (mm < UNP) { xn[2 * mm + h][e] = unpk(xr[2 * mm + h][e]); PIN(xn[2 * mm + h][e]); }
        }
        t[h] = fold32(s1, s2);
      }
      u[mm] = dpp_add<0x121>(dpp_add<0x122>(dpp_add<0x124>(dpp_add<0x128>(fold16(t[0], t[1])))));
      PIN(u[mm]);
    }
    CPF_USE();
    LAS unsigned char* stq = L + L_ST + q * 512;
    if (r == 0) {
      *(LAS f32x4*)(stq + g * 48) = f32x4{u[0], u[1], u[2], u[3]};
      *(LAS f32x4*)(stq + g * 48 + 16) = f32x4{u[4], u[5], u[6], u[7]};
      *(LAS f32x2*)(stq + g * 48 + 32) = f32x2{u[8], u[9]};
    }
    {
      const int rw = min(lane, 19);
      const float mu = *(LAS float*)(stq + (rw & 1) * 48 + (rw >> 1) * 4) * (1.f / D), sq = *(LAS float*)(stq + 96 + (rw & 1) * 48 + (rw >> 1) * 4);
      const float rstd = __builtin_amdgcn_rsqf(fmaxf(sq * (1.f / D) - mu * mu, 0.f) + 1e-5f);
      *(LAS f32x4*)(stq + 192 + rw * 16) = f32x4{rstd, rstd, -mu * rstd, -mu * rstd};
    }
#pragma unroll
    for (int t = 2 * UNP; t < 20; ++t)
#pragma unroll
      for (int e = 0; e < 4; ++e) { xn[t][e] = unpk(xr[t][e]); PIN(xn[t][e]); }
    {
      f32x4 st[2][4];
#pragma unroll
      for (int t = 0; t < 4; ++t) st[0][t] = *(LAS f32x4*)(stq + 192 + t * 16);
#pragma unroll
      for (int t4 = 0; t4 < 5; ++t4) {
        if (t4 < 4) {
#pragma unroll
          for (int t = 0; t < 4; ++t) st[(t4 + 1) & 1][t] = *(LAS f32x4*)(stq + 192 + (t4 * 4 + 4 + t) * 16);
        }
        SCHED();
#pragma unroll
        for (int t = 0; t < 4; ++t)
#pragma unroll
          for (int e = 0; e < 4; ++e) {
            xn[t4 * 4 + t][e] = fma2(xn[t4 * 4 + t][e], f32x2{st[t4 & 1][t].x, st[t4 & 1][t].y}, f32x2{st[t4 & 1][t].z, st[t4 & 1][t].w});
            PIN(xn[t4 * 4 + t][e]);
          }
      }
    }
    unsigned hb0, hb1;
    {
      const int ks = lc >> 2, gg = lc & 3;
      const int slot = ks;
      const unsigned base = L_HT + (slot * 64 + gg * 16 + ((4 * slot + gg) & 7)) * 16;
      hb0 = (base ^ (cq << 4)) + (q + 1) * 14336;
      hb1 = base ^ (q << 6);
    }
    RETRY(MOD_BAD, MOD_LOAD(0));
    MOD_PUT();
    LGKM0();
    if (lane == 0) ARRIVE(2);
    CNT_WAIT(2, 4u);
    if (lane < 56) {
      const f32x4 s0 = *(LAS f32x4*)(L + L_MOD + lc * 32), s1 = *(LAS f32x4*)(L + L_MOD + lc * 32 + 16);
      const f32x4 c0 = *(LAS f32x4*)(L + L_MOD + 1792 + lc * 32), c1 = *(LAS f32x4*)(L + L_MOD + 1792 + lc * 32 + 16);
      const f32x2 sf[4] = {{s0.x, s0.y}, {s0.z, s0.w}, {s1.x, s1.y}, {s1.z, s1.w}};
      const f32x2 cf[4] = {{c0.x, c0.y}, {c0.z, c0.w}, {c1.x, c1.y}, {c1.z, c1.w}};
#pragma unroll
      for (int i = 0; i < 20; ++i) {
        v4u h;
#pragma unroll
        for (int e = 0; e < 4; ++e) { const f32x2 v = fma2(xn[i][e], cf[e], sf[e]); h[e] = pack2(v.x, v.y); }
        *(LAS v4u*)(L + (i < 16 ? hb0 ^ (i << 4) : hb1 ^ ((i - 16) << 4))) = h;
      }
    }
    v4u rd = pz;
    if (w == 7) rd = LD128(lane * 128, WS_READY);
    BAR();
    __builtin_amdgcn_s_setprio(3);
    {
      const int pv = min(lane + 64, 111) * 16, po = (p * 4 + q) * 14336 + j * 1792;
      ST128(pz, lane * 16, WS_OBUF + po); ST128(pz, pv, WS_OBUF + po);
      ST128(pz, lane * 16, WS_X1 + po); ST128(pz, pv, WS_X1 + po);
      if (q == 0) ST128(pz, g * 2048 + r * 16, WS_ST + p * 8192 + j * 256);
    }
    v4u m4, m5;
    const int gso = WS_MODG + ((w & 1 ? 5 : 2) * 448 + 56 * j) * 8;
#define G_LOAD() do { MOD_LOAD(3); m4 = LD128(min(lane, 27) * 16, gso); m5 = LD128(min(lane, 27) * 16, gso + 2688 * 8); } while (0)
    G_LOAD();
    SCHED();
    const int to = (((w >> 1) & 1) + 1) * 14336;
    f32x4 a0[2], a1[2];
    v4u bc[2][2];
#pragma unroll
    for (int t = 0; t < 2; ++t) {
      a0[t] = f32x4{0, 0, 0, 0}; a1[t] = f32x4{0, 0, 0, 0};
      bc[0][t] = *(LAS v4u*)(hre + to + t * 28672); bc[1][t] = *(LAS v4u*)(hro + to + t * 28672 + 1024);
    }
    LAS unsigned char* qw = L + L_WO + head * 25088 + fr14;
    v4u wa2[2], wb2[2];
#pragma unroll
    for (int s = 0; s < 2; ++s) { wa2[s] = *(LAS v4u*)(qw + s * 1792); wb2[s] = *(LAS v4u*)(qw + s * 1792 + 896); }
    SCHED();
#pragma unroll
    for (int s = 0; s < 14; ++s) {
#pragma unroll
      for (int t = 0; t < 2; ++t) {
        a0[t] = mfma(wa2[s & 1], bc[s & 1][t], a0[t]);
        a1[t] = mfma(wb2[s & 1], bc[s & 1][t], a1[t]);
        if (s < 12) bc[s & 1][t] = *(LAS v4u*)(((s & 1) ? hro : hre) + to + t * 28672 + (s + 2) * 1024);
      }
      if (s < 12) { wa2[s & 1] = *(LAS v4u*)(qw + (s + 2) * 1792); wb2[s & 1] = *(LAS v4u*)(qw + (s + 2) * 1792 + 896); }
      if (s & 1) ST128(pz, lane * 16 + ((s >> 1) & 3) * 1024, WS_H + (((p * 4 + q) * 8 + j) * 7 + ((s >> 1) & 4)) * 1024);
      SCHED();
    }
    USE(m5);
    if (lane == 0) { if (ARRIVE(5) == 3u) ST32(tag, WS_RP + (p * 8 + j) * 128, 0); }
    v4u q0;
#pragma unroll
    for (int e = 0; e < 4; ++e) { q0[e] = pack2(a0[0][e], a1[0][e]); qmine[e] = pack2(a0[1][e], a1[1][e]); }
    if (g == 3) { q0[2] = 0u; q0[3] = 0u; qmine[2] = 0u; qmine[3] = 0u; }
    *(LAS v4u*)(L + L_QF + q * 1024 + lane * 16) = q0;
    if (w == 7) {
      RETRY(((rd.x ^ tag) | (rd.y ^ tag) | (rd.z ^ tag) | (rd.w ^ tag)) != 0u, __builtin_amdgcn_s_sleep(8); rd = LD128(lane * 128, WS_READY));
      if (lane == 0) {
        ARRIVE(7);
        if (b == 0) ST32(tag, WS_GEN, 0);
      }
    }
    CNT_WAIT(7, 1u);
#pragma unroll
    for (int u = 0; u < 13; ++u) wt[u] = LD128(lane * 16 + (u & 3) * 1024, WS_PO + j * 50176 + (wo0 + (u & 12)) * 1024);
    __builtin_amdgcn_s_setprio(1);
    if (w < 7) {
#pragma unroll
      for (int gi = 0; gi < QP1; ++gi) P1_LOAD4(gi);
    }
    SCHED();
    MK_BIAS();
    RETRY(MOD_BAD || ((m4.y ^ tag) | (m4.w ^ tag) | (m5.y ^ tag) | (m5.w ^ tag)) != 0u, G_LOAD());
    MOD_PUT();
    {
      const unsigned k = pack2(asf(m4.x) + asf(m5.x), asf(m4.z) + asf(m5.z));
      if (lane < 28 && w < 6) {
        if (w & 1) *(LAS f32x2*)(L + L_G + 256 + lane * 8) = unpk(k);
        else { *(LAS float*)(L + L_G + (lane >> 2) * 32 + (lane & 3) * 4) = bflo(k); *(LAS float*)(L + L_G + (lane >> 2) * 32 + 16 + (lane & 3) * 4) = bfhi(k); }
      }
    }
  }
  __builtin_amdgcn_s_setprio(3);
  const int colh = 28 * (2 * j + head) + 4 * g;
  const int oa0 = r * 16 + (colh >> 3) * 256 + ((colh >> 2) & 1) * 8;
  BAR();
  unsigned rp = __builtin_amdgcn_raw_buffer_load_b32(rWS, (lane & 7) * 128, WS_RP + p * 1024, SC1);
  unsigned t_pub;
  {
    if (w < 4) qmine = *(LAS v4u*)(L + L_QF + w * 1024 + lane * 16);
    LAS unsigned char* kvb = L + L_KV + w * 2048 + lane * 16;
    const v4u kp = *(LAS v4u*)(kvb), kc = *(LAS v4u*)(kvb + 4096);
    LAS unsigned char* vtb = L + L_KV + w * 2048 + 1024 + (4 * g + (r >> 2)) * 64 + (r & 3) * 8;
    const v2u vp0 = trread(vtb), vp1 = trread(vtb + 32), vc0 = trread(vtb + 4096), vc1 = trread(vtb + 4096 + 32);
    const f32x4 z = {0, 0, 0, 0};
    const f32x4 s0 = mfma(kp, qmine, bias0), s1 = mfma(kc, qmine, bias1);
    const float mx = rows_max(fmaxf(fmaxf(fmaxf(s0[0], s0[1]), fmaxf(s0[2], s0[3])), fmaxf(fmaxf(s1[0], s1[1]), fmaxf(s1[2], s1[3]))));
    const float cs = 0.18898223650461362f * 1.4426950408889634f;
    const float mc = -mx * cs;
    float pv[8];
#pragma unroll
    for (int e = 0; e < 4; ++e) { pv[e] = __builtin_amdgcn_exp2f(s0[e] * cs + mc); pv[4 + e] = __builtin_amdgcn_exp2f(s1[e] * cs + mc); }
    v4u pf;
#pragma unroll
    for (int e = 0; e < 4; ++e) pf[e] = pack2(pv[2 * e], pv[2 * e + 1]);
    const f32x4 o0 = mfma(v4u{vp0.x, vp0.y, vc0.x, vc0.y}, pf, z), o1 = mfma(v4u{vp1.x, vp1.y, vc1.x, vc1.y}, pf, z);
    const float inv = __builtin_amdgcn_rcpf(rows_sum(((pv[0] + pv[1]) + (pv[2] + pv[3])) + ((pv[4] + pv[5]) + (pv[6] + pv[7]))));
    const int ob = WS_OBUF + (p * 4 + T_) * 14336;
    v2u d0 = {sane(pack2(o0[0] * inv, o0[1] * inv)), sane(pack2(o0[2] * inv, o0[3] * inv))};
    v2u d1 = {sane(pack2(o1[0] * inv, o1[1] * inv)), sane(pack2(o1[2] * inv, o1[3] * inv))};
    ST64(d0, oa0, ob);
    if (g < 3) ST64(d1, oa0 + 512, ob);
    t_pub = now32();
  }
  {
    const int kh = w & 1;
    __builtin_amdgcn_s_setprio(0);
    const v4u xv = __builtin_amdgcn_raw_buffer_load_b128(rX, r * 896 + min(kh * 4 + g, 6) * 16, (64 * p + 16 * T_) * 896 + 112 * j, 0);
    if (w < 7) {
#pragma unroll
      for (int gi = QP1; gi < QP2; ++gi) P1_LOAD4(gi);
    }
    SCHED();
    LAS unsigned char* wa_ = L + L_WO + kh * 25088 + lane * 16;
    LAS unsigned char* wb_ = L + L_WO + kh * 25088 + 2048 + (g * 12 + min(r, 11)) * 16;
    v4u wq[3][4];
#define WO_READ(s) do { wq[(s) % 3][0] = *(LAS v4u*)(wa_ + (s) * 3584); wq[(s) % 3][1] = *(LAS v4u*)(wa_ + (s) * 3584 + 1024); \
    wq[(s) % 3][2] = *(LAS v4u*)(wb_ + (s) * 3584); wq[(s) % 3][3] = *(LAS v4u*)(wb_ + (s) * 3584 + 768); } while (0)
    RETRY(rp != tag, rp = __builtin_amdgcn_raw_buffer_load_b32(rWS, (lane & 7) * 128, WS_RP + p * 1024, SC1));
    if (w >= 4) {
#pragma unroll
      for (int u = 0; u < 13; ++u) *(LAS v4u*)(L + L_WO + (wo0 + u) * 1024 + lane * 16) = wt[u];
      LGKM0();
      if (lane == 0) ARRIVE(6);
    }
    pace(t_pub, DLY_O);
    __builtin_amdgcn_s_setprio(3);
    v4u of[7];
#define O_LOAD() do { _Pragma("unroll") for (int s = 0; s < 7; ++s) of[s] = LD128(lane * 16 + (s & 3) * 1024, WS_OBUF + (p * 4 + T_) * 14336 + (kh * 7 + (s & 4)) * 1024); } while (0)
    O_LOAD();
    SCHED();
    CNT_WAIT(6, 4u);
    WO_READ(0); WO_READ(1); WO_READ(2);
    const f32x4 ge = *(LAS f32x4*)(L + L_G + kh * 128 + g * 32), go = *(LAS f32x4*)(L + L_G + kh * 128 + g * 32 + 16);
    f32x4 xe = {bflo(xv[0]), bflo(xv[1]), bflo(xv[2]), bflo(xv[3])}, xo4 = {bfhi(xv[0]), bfhi(xv[1]), bfhi(xv[2]), bfhi(xv[3])};
    PIN(xe); PIN(xo4);
#define MAX7(a) max4(max4(max4(max4(max4(max4(max4(0u, a[0]), a[1]), a[2]), a[3]), a[4]), a[5]), a[6])
    RETRY(MAX7(of) == POISON, O_LOAD());
    f32x4 acc[4];
#pragma unroll
    for (int a = 0; a < 4; ++a) acc[a] = f32x4{0, 0, 0, 0};
#pragma unroll
    for (int s = 0; s < 7; ++s) {
#pragma unroll
      for (int a = 0; a < 4; ++a) acc[a] = mfma(wq[s % 3][a], of[s], acc[a]);
      if (s + 3 < 7) WO_READ(s + 3);
      if (w < 7 && !(s & 1) && QP2 + (s >> 1) < QP3) P1_LOAD4(QP2 + (s >> 1));
      SCHED();
    }
    f32x4 y0, y1;
    LAS unsigned char* ox = L + L_OX + lane * 16;
    if (kh) {
      *(LAS f32x4*)(ox + w * 2048) = acc[0]; *(LAS f32x4*)(ox + w * 2048 + 1024) = acc[1];
      y0 = acc[2]; y1 = acc[3];
    } else {
      *(LAS f32x4*)(ox + w * 2048) = acc[2]; *(LAS f32x4*)(ox + w * 2048 + 1024) = acc[3];
      y0 = acc[0]; y1 = acc[1];
    }
    BAR();
    y0 += *(LAS f32x4*)(ox + (w ^ 1) * 2048);
    y1 += *(LAS f32x4*)(ox + (w ^ 1) * 2048 + 1024);
    const bool live = !(kh && g == 3);
    y0 = __builtin_elementwise_fma(ge, y0, xe);
    y1 = __builtin_elementwise_fma(go, y1, xo4);
    v4u y;
#pragma unroll
    for (int e = 0; e < 4; ++e) y[e] = sane(pack2(y0[e], y1[e]));
    if (live) {
      ST128(y, lane * 16, WS_X1 + (p * 4 + T_) * 14336 + j * 1792 + kh * 1024);
      *(LAS v4u*)(L + L_X1 + T_ * 1792 + kh * 1024 + lane * 16) = y;
    }
    const f32x4 sm = y0 + y1, sq = __builtin_elementwise_fma(y0, y0, y1 * y1);
    float s1 = (sm[0] + sm[1]) + (sm[2] + sm[3]), s2 = (sq[0] + sq[1]) + (sq[2] + sq[3]);
    if (!live) { s1 = 0.f; s2 = 0.f; }
    float sv = fold32(s1, s2);
    sv = fold16(sv, sv);
    if (!(g & 1)) ST32(sane(asu(sv)), r * 16 + (g >> 1) * 4, WS_ST + ((p * 4 + T_) * 8 + j) * 256 + kh * 8);
    t_pub = now32();
  }

  const int p2_base = WS_P2 + (j * 56 + w * 7) * 3584;
  const int fr12 = 2048 + (g * 12 + min(r, 11)) * 16;
#define P2_LOAD4(d0, d1, d2, d3, s) do { const int so_ = p2_base + (s) * 3584; \
    d0 = LD128(lane * 16, so_); d1 = LD128(lane * 16 + 1024, so_); d2 = LD128(fr12, so_); d3 = LD128(fr12 + 768, so_); } while (0)
#define WE_LOAD(s) P2_LOAD4(we[s][0], we[s][1], we[s][2], we[s][3], s)
#define WL_LOAD(q) P2_LOAD4(fa[2 * (q)], fb[2 * (q)], fa[2 * (q) + 1], fb[2 * (q) + 1], W2E + (q))
#define H_LOAD1(dst, T, s) do { dst[s] = LD128(lane * 16, WS_H + (((p * 4 + (T)) * 8 + w) * 7 + (s)) * 1024); } while (0)
#define H_LOAD(dst, T) do { _Pragma("unroll") for (int s = 0; s < 7; ++s) H_LOAD1(dst, T, s); } while (0)
  v4u we[W2E][4], h0[7], h1[7];
  const unsigned cnt0 = mtask ? 11u : 7u;
  __builtin_amdgcn_s_setprio(0);
  if (w < 7) {
#pragma unroll
    for (int gi = QP3; gi < QP4; ++gi) P1_LOAD4(gi);
    const int sg = w;
    LAS unsigned char* h2w = L + L_HT + 14336 + sg * 2048 + lane * 16;
    LAS unsigned char* h2r = L + L_HT + 14336 + lane * 16;
    LAS unsigned char* mc2 = L + L_MOD + w * 256 + g * 32;
    v4u xa[4][2];
#define X1_LOAD1(t) do { const int so_ = WS_X1 + (p * 4 + (t)) * 14336 + w * 2048; xa[t][0] = LD128(lane * 16, so_); xa[t][1] = LD128(lane * 16 + 1024, so_); } while (0)
#define X1_LOAD() do { _Pragma("unroll") for (int t = 0; t < 4; ++t) { const int so_ = WS_X1 + (p * 4 + t) * 14336 + w * 2048; \
      xa[t][0] = LD128(lane * 16, so_); xa[t][1] = LD128(lane * 16 + 1024, so_); } } while (0)
    f32x4 cs_[2][4];
#pragma unroll
    for (int i = 0; i < 2; ++i) {
      cs_[i][0] = *(LAS f32x4*)(mc2 + i * 128); cs_[i][1] = *(LAS f32x4*)(mc2 + i * 128 + 16);
      cs_[i][2] = *(LAS f32x4*)(mc2 + 1792 + i * 128); cs_[i][3] = *(LAS f32x4*)(mc2 + 1792 + i * 128 + 16);
    }
    pace(t_pub, DLY_U + 150);
    __builtin_amdgcn_s_setprio(2);
    X1_LOAD();
    SCHED();
    BAR();
#pragma unroll
    for (int t = 0; t < 4; ++t) {
      const f32x4 st = *(LAS f32x4*)(L + L_ST + (t * 16 + r) * 16);
      const f32x2 a_ = {st.x, st.y}, b_ = {st.z, st.w};
      RETRY(max4(max4(0u, xa[t][0]), xa[t][1]) == POISON, _Pragma("unroll") for (int u = t; u < 4; ++u) X1_LOAD1(u));
      if (t == 3) {
#pragma unroll
        for (int gi = QP4; gi < 7; ++gi) P1_LOAD4(gi);
#pragma unroll
        for (int q = 0; q < WE0; ++q) WE_LOAD(q);
        SCHED();
      }
#pragma unroll
      for (int i = 0; i < 2; ++i) {
        const f32x2 v0 = fma2(fma2(unpk(xa[t][i][0]), a_, b_), f32x2{cs_[i][2].x, cs_[i][2].y}, f32x2{cs_[i][0].x, cs_[i][0].y});
        const f32x2 v1 = fma2(fma2(unpk(xa[t][i][1]), a_, b_), f32x2{cs_[i][2].z, cs_[i][2].w}, f32x2{cs_[i][0].z, cs_[i][0].w});
        const f32x2 v2 = fma2(fma2(unpk(xa[t][i][2]), a_, b_), f32x2{cs_[i][3].x, cs_[i][3].y}, f32x2{cs_[i][1].x, cs_[i][1].y});
        const f32x2 v3 = fma2(fma2(unpk(xa[t][i][3]), a_, b_), f32x2{cs_[i][3].z, cs_[i][3].w}, f32x2{cs_[i][1].z, cs_[i][1].w});
        *(LAS v4u*)(h2w + t * 14336 + i * 1024) = v4u{pack2(v0.x, v0.y), pack2(v1.x, v1.y), pack2(v2.x, v2.y), pack2(v3.x, v3.y)};
      }
    }
    BAR();
    __builtin_amdgcn_s_setprio(1);
    f32x4 a0[4], a1[4];
    v4u bf[UD][2], hv[2];
#define SILU1(t, e) do { const f32x2 u_ = {a0[t][e], a1[t][e]}; \
      const f32x2 t_ = u_ * f32x2{-1.4426950408889634f, -1.4426950408889634f}; \
      const f32x2 d_ = f32x2{__builtin_amdgcn_exp2f(t_.x), __builtin_amdgcn_exp2f(t_.y)} + f32x2{1.f, 1.f}; \
      const f32x2 o_ = u_ * f32x2{__builtin_amdgcn_rcpf(d_.x), __builtin_amdgcn_rcpf(d_.y)}; \
      hv[(t) & 1][e] = sane(pack2(o_.x, o_.y)); } while (0)
#define H_STORE(t) ST128(hv[(t) & 1], lane * 16, WS_H + (((p * 4 + (t)) * 8 + j) * 7 + w) * 1024)
#pragma unroll
    for (int t = 0; t < 4; ++t) { a0[t] = f32x4{0, 0, 0, 0}; a1[t] = f32x4{0, 0, 0, 0}; }
#pragma unroll
    for (int u = 0; u < UD; ++u) { bf[u][0] = *(LAS v4u*)(h2r + u * 1024); bf[u][1] = *(LAS v4u*)(h2r + 14336 + u * 1024); }
    SCHED();
#pragma unroll
    for (int ss = 0; ss < 28; ++ss) {
      const int P = ss / 14, s = ss % 14, n = ss + UD;
#pragma unroll
      for (int t = 0; t < 2; ++t) {
        a0[2 * P + t] = mfma(fa[s], bf[ss % UD][t], a0[2 * P + t]);
        a1[2 * P + t] = mfma(fb[s], bf[ss % UD][t], a1[2 * P + t]);
        if (n < 28) bf[ss % UD][t] = *(LAS v4u*)(h2r + (2 * (n / 14) + t) * 14336 + (n % 14) * 1024);
      }
      if (P == 0 && (s & 3) == 1 && WE0 + (s >> 2) < W2E) WE_LOAD(WE0 + (s >> 2));
      if (P == 1) {
        if (s >= 1 && s < 9) SILU1((s - 1) >> 2, (s - 1) & 3);
        if (s == 4) H_STORE(0);
        if (s == 8) { H_STORE(1); if (lane == 0) ARRIVE(0); }
        if (s >= 9 && s - 9 < 7 - W2E) WL_LOAD(s - 9);
      }
      SCHED();
    }
#pragma unroll
    for (int e = 0; e < 4; ++e) { SILU1(2, e); SILU1(3, e); }
    H_STORE(2); H_STORE(3);
    if (lane == 0) ARRIVE(1);
  } else {
    v4u st[8];
#define ST_LOAD() do { _Pragma("unroll") for (int q = 0; q < 8; ++q) st[q] = LD128(g * 2048 + r * 16 + q * 256, WS_ST + p * 8192); } while (0)
    pace(t_pub, DLY_U);
    __builtin_amdgcn_s_setprio(3);
    ST_LOAD();
    RETRY(max4(MAX7(st), st[7]) == POISON, ST_LOAD());
    f32x4 a = __builtin_bit_cast(f32x4, st[0]);
#pragma unroll
    for (int q = 1; q < 8; ++q) a += __builtin_bit_cast(f32x4, st[q]);
    const float mu = (a.x + a.z) * (1.f / D);
    const float rstd = __builtin_amdgcn_rsqf(fmaxf((a.y + a.w) * (1.f / D) - mu * mu, 0.f) + 1e-5f);
    *(LAS f32x4*)(L + L_ST + lane * 16) = f32x4{rstd, rstd, -mu * rstd, -mu * rstd};
    __builtin_amdgcn_s_setprio(0);
    BAR();
#pragma unroll
    for (int s = 0; s < W2E; ++s) WE_LOAD(s);
    BAR();
#pragma unroll
    for (int q = 0; q < 7 - W2E; ++q) WL_LOAD(q);
  }
  if (w < 4 || w == 7) {
    CNT_WAIT(0, cnt0);
    pace(now32(), DLY_H);
  }
  H_LOAD(h0, 0); H_LOAD(h1, 1);

#define W2F(s, a) ((s) < W2E ? we[(s) < W2E ? (s) : 0][a] : (((a) & 1) ? fb[2 * ((s) >= W2E ? (s) - W2E : 0) + ((a) >> 1)] : fa[2 * ((s) >= W2E ? (s) - W2E : 0) + ((a) >> 1)]))
#define RED_WRITE(red, acc) do { _Pragma("unroll") for (int a = 0; a < 4; ++a) { *(LAS f32x2*)((red) + (w * 4 + a) * 1024 + lane * 8) = f32x2{acc[a][0], acc[a][1]}; *(LAS f32x2*)((red) + (w * 4 + a) * 1024 + 512 + lane * 8) = f32x2{acc[a][2], acc[a][3]}; } } while (0)
#define OUT_STORE(y, T, q4) do { if ((q4) < 2 || g < 3) __builtin_amdgcn_raw_buffer_store_b64(y, rOUT, r * 896 + g * 16, (64 * p + 16 * (T)) * 896 + 112 * j + ((q4) >> 1) * 64 + ((q4) & 1) * 8, 0); } while (0)
#define RED_FINISH(yo, red, T, q4) do { \
    LAS unsigned char* rb_ = (red) + ((q4) >> 1) * 2048 + ((q4) & 1) * 512 + lane * 8; \
    f32x2 s0 = *(LAS f32x2*)(rb_), s1 = *(LAS f32x2*)(rb_ + 1024); \
    _Pragma("unroll") for (int ww = 1; ww < 8; ++ww) { s0 += *(LAS f32x2*)(rb_ + ww * 4096); s1 += *(LAS f32x2*)(rb_ + ww * 4096 + 1024); } \
    const v2u x1 = *(LAS v2u*)(L + L_X1 + (T) * 1792 + min(((q4) >> 1) * 4 + g, 6) * 256 + r * 16 + ((q4) & 1) * 8); \
    const f32x4 gb = *(LAS f32x4*)(L + L_G + 256 + ((q4) >> 1) * 128 + g * 32 + ((q4) & 1) * 16); \
    yo = v2u{pack2(bflo(x1.x) + gb.x * s0.x, bfhi(x1.x) + gb.y * s1.x), pack2(bflo(x1.y) + gb.z * s0.y, bfhi(x1.y) + gb.w * s1.y)}; \
  } while (0)
#define D_TILE(acc, h, EXTRA) do { \
    _Pragma("unroll") for (int a = 0; a < 4; ++a) acc[a] = f32x4{0, 0, 0, 0}; \
    _Pragma("unroll") for (int s = 0; s < 7; ++s) { \
      _Pragma("unroll") for (int a = 0; a < 4; ++a) acc[a] = mfma(W2F(s, a), h[s], acc[a]); \
      EXTRA; SCHED(); } } while (0)
  {
    v4u h2[7], h3[7];
    SCHED();
    f32x4 ac[4], ad[4];
#define RW1(red, acc, a) do { *(LAS f32x2*)((red) + (w * 4 + (a)) * 1024 + lane * 8) = f32x2{acc[a][0], acc[a][1]}; *(LAS f32x2*)((red) + (w * 4 + (a)) * 1024 + 512 + lane * 8) = f32x2{acc[a][2], acc[a][3]}; } while (0)
    unsigned mx;
    RETRY(MAX7(h0) == POISON, H_LOAD(h0, 0));
    mx = 0u;
    D_TILE(ac, h0, mx = max4(mx, h1[s]));
    RETRY2(mx == POISON, MAX7(h1) == POISON, H_LOAD(h1, 1));
    CNT_WAIT(1, 11u);
    D_TILE(ad, h1, if (s >= 1 && s < 5) RW1(L + L_R01, ac, s - 1); H_LOAD1(h2, 2, s));
    H_LOAD(h3, 3);
    RETRY(MAX7(h2) == POISON, H_LOAD(h2, 2));
    mx = 0u;
    D_TILE(ac, h2, if (s >= 1 && s < 5) RW1(L + L_R01 + 32768, ad, s - 1); mx = max4(mx, h3[s]));
    RETRY2(mx == POISON, MAX7(h3) == POISON, H_LOAD(h3, 3));
    v2u y01, y23;
    D_TILE(ad, h3, if (s >= 1 && s < 5) RW1(L + L_R23, ac, s - 1));
    RED_WRITE(L + L_R23 + 32768, ad);
    BAR();
    RED_FINISH(y01, L + L_R01 + (w >> 2) * 32768, (w >> 2), (w & 3));
    RED_FINISH(y23, L + L_R23 + (w >> 2) * 32768, 2 + (w >> 2), (w & 3));
    OUT_STORE(y01, (w >> 2), (w & 3));
    OUT_STORE(y23, 2 + (w >> 2), (w & 3));
  }
  asm volatile("adaln_lbl1:");
}

extern "C" void kernel_launch(void* const* d_in, const int* in_sizes, int n_in, void* d_out, int out_size, void* d_ws,
                              size_t ws_size, hipStream_t stream) {
  adaln_block<<<256, 512, 0, stream>>>((const u16*)d_in[0], (const u16*)d_in[1], (const u16*)d_in[2], (const u16*)d_in[4],
                                       (const u16*)d_in[5], (const u16*)d_in[6], (const u16*)d_in[7], (u16*)d_out,
                                       (unsigned char*)d_ws);
}
```
